```python
import torch

def _softmax(x, axis=-1):
    return torch.softmax(x, dim=axis)
def _gelu(x):
    return torch.nn.functional.gelu(x, approximate="tanh")
def _silu(x):
    return torch.nn.functional.silu(x)
def _sigmoid(x):
    return torch.sigmoid(x)
def _layernorm(x, axis=-1, eps=1e-5):
    m = x.mean(dim=axis, keepdim=True)
    v = x.var(dim=axis, keepdim=True, unbiased=False)
    return (x - m) / torch.sqrt(v + eps)
def _scatter_add(tgt, idx, src, axis=0):
    return tgt.clone().index_add_(axis, idx, src)
def _segment_sum(src, seg, n):
    out = torch.zeros((n,) + src.shape[1:], dtype=src.dtype)
    return out.index_add_(0, seg, src)
def _pad(x, pw):
    flat = tuple(v for pair in reversed(pw) for v in pair)
    return torch.nn.functional.pad(x, flat)

kernel_name = 'concat_heads__sliding_local_dilated__softmax_full_mha__prompt'

def setup_inputs():

    _w = (lambda a: (lambda t: t.to(torch.bfloat16) if t.is_floating_point() else t)(torch.as_tensor(a)))
    def _A_setup():
        B, S, D = 1, 2048, 448
        return {
            "x": _w((lambda *s: torch.randn(*s, dtype=torch.bfloat16) * 0.4)(B, S, D)),
            "wqkv": _w((lambda *s: torch.randn(*s, dtype=torch.bfloat16) * 0.8)(D, 3 * D)),
            "wo": _w((lambda *s: torch.randn(*s, dtype=torch.bfloat16) * 0.1)(D, D)),
        }
    _A_in = _A_setup()

    def _B_setup():
        B, S, D = 1, 2048, 448
        return {
            "x": _w((lambda *s: torch.randn(*s, dtype=torch.bfloat16) * 0.2)(B, S, D)),
            "wqkv": _w((lambda *s: torch.randn(*s, dtype=torch.bfloat16) * 0.8)(D, 3 * D)),
            "wo": _w((lambda *s: torch.randn(*s, dtype=torch.bfloat16) * 0.2)(D, D)),
        }
    _B_in = _B_setup()

    return {
        "A_x": _A_in["x"],
        "A_wqkv": _A_in["wqkv"],
        "A_wo": _A_in["wo"],
        "B_x": _B_in["x"],
        "B_wqkv": _B_in["wqkv"],
        "B_wo": _B_in["wo"],
    }

def reference(A_x, A_wqkv, A_wo, B_x, B_wqkv, B_wo):
    def _A_ref(x, wqkv, wo):
        B, S, D = x.shape
        H = 16; Dh = D // H
        W, dil = 8, 2
        qkv = (lambda x, *d: x.permute(*d))((x @ wqkv).reshape(B, S, 3, H, Dh), 2, 0, 3, 1, 4)
        q, k, v = qkv[0], qkv[1], qkv[2]
        scale = Dh ** -0.5
        pos = torch.arange(S)
        off = torch.arange(W) * dil
        idx = pos[:, None] - off[None, :]
        mask = idx >= 0
        idx_c = torch.where(mask, idx, 0).reshape(-1)
        kg = k[:, :, idx_c, :].reshape(B, H, S, W, Dh)
        vg = v[:, :, idx_c, :].reshape(B, H, S, W, Dh)
        logits = torch.einsum("bhsd,bhswd->bhsw", q, kg) * scale
        logits = torch.where(mask[None, None, :, :], logits, -1e9)
        attn = _softmax(logits, axis=-1)
        o = torch.einsum("bhsw,bhswd->bhsd", attn, vg)
        return (lambda x, *d: x.permute(*d))(o, 0, 2, 1, 3).reshape(B, S, D) @ wo

    def _B_ref(x, wqkv, wo):
        B, S, D = x.shape
        H = 16; Dh = D // H
        qkv = (lambda x, *d: x.permute(*d))((x @ wqkv).reshape(B, S, 3, H, Dh), 2, 0, 3, 1, 4)
        q, k, v = qkv[0], qkv[1], qkv[2]
        scale = Dh ** -0.5
        scores = torch.einsum("bhqd,bhkd->bhqk", q, k) * scale
        p = _softmax(scores, axis=-1)
        o = torch.einsum("bhqk,bhkd->bhqd", p, v)
        return (lambda x, *d: x.permute(*d))(o, 0, 2, 1, 3).reshape(B, S, D) @ wo

    a = _A_ref(A_x, A_wqkv, A_wo)
    b = _B_ref(B_x, B_wqkv, B_wo)
    return torch.concatenate([a, b], axis=-1)
```

```cpp
#include <hip/hip_runtime.h>
#include <hip/hip_bf16.h>
typedef __hip_bfloat16 bf16;
#define S_ 2048
#define D_ 448
#define H_ 16
#define DH 28
__device__ inline float b2f(bf16 x){return __bfloat162float(x);}
__global__ void gemm_naive(const bf16* A, const bf16* B, bf16* C, int M, int N, int K, int ldc){
  int n = blockIdx.x*64 + threadIdx.x; int m = blockIdx.y*4 + threadIdx.y;
  if(n>=N||m>=M) return;
  float acc=0;
  for(int k=0;k<K;++k) acc += b2f(A[m*K+k])*b2f(B[k*N+n]);
  C[m*ldc+n] = __float2bfloat16(acc);
}
#define MIMIC 0
__device__ inline float rb(float x){ return MIMIC? b2f(__float2bfloat16(x)) : x; }
__global__ void local_attn(const bf16* qkv, bf16* o){
  int s = blockIdx.x*4+threadIdx.y; int h = threadIdx.x; if(h>=16) return;
  const float scale = 0.18898223650461362f;
  float lg[8]; float mx=-1e30f;
  #pragma unroll
  for(int w=0;w<8;++w){ int idx=s-2*w; if(idx>=0){ float a=0; for(int d=0;d<DH;++d) a+=b2f(qkv[s*1344+h*28+d])*b2f(qkv[idx*1344+448+h*28+d]); lg[w]=rb(rb(a)*scale);} else lg[w]=-1e9f; mx=fmaxf(mx,lg[w]); }
  float sum=0;
  #pragma unroll
  for(int w=0;w<8;++w){ lg[w]=__expf(lg[w]-mx); sum+=lg[w]; }
  for(int d=0;d<DH;++d){ float a=0;
    #pragma unroll
    for(int w=0;w<8;++w){ int idx=s-2*w; if(idx<0) idx=0; a+=rb(lg[w]/sum)*b2f(qkv[idx*1344+896+h*28+d]); }
    o[s*448+h*28+d]=__float2bfloat16(a);
  }
}
__global__ void full_attn(const bf16* qkv, bf16* o){
  __shared__ float sc[2048]; __shared__ float red[256]; __shared__ float q[28];
  int s=blockIdx.x, h=blockIdx.y, t=threadIdx.x;
  const float scale = 0.18898223650461362f;
  if(t<28) q[t]=b2f(qkv[s*1344+h*28+t]);
  __syncthreads();
  float mx=-1e30f;
  for(int k=t;k<2048;k+=256){ float a=0; for(int d=0;d<28;++d) a+=q[d]*b2f(qkv[k*1344+448+h*28+d]); a=rb(rb(a)*scale); sc[k]=a; mx=fmaxf(mx,a);}
  red[t]=mx; __syncthreads();
  for(int o2=128;o2>0;o2>>=1){ if(t<o2) red[t]=fmaxf(red[t],red[t+o2]); __syncthreads(); }
  mx=red[0]; __syncthreads();
  float sum=0;
  for(int k=t;k<2048;k+=256){ float e=__expf(sc[k]-mx); sc[k]=e; sum+=e; }
  red[t]=sum; __syncthreads();
  for(int o2=128;o2>0;o2>>=1){ if(t<o2) red[t]+=red[t+o2]; __syncthreads(); }
  sum=red[0]; __syncthreads();
  int d=t&31, part=t>>5; float a=0;
  if(d<28) for(int k=part;k<2048;k+=8) a+=rb(sc[k]/sum)*b2f(qkv[k*1344+896+h*28+d]);
  red[t]=a; __syncthreads();
  if(t<28){ float r=0; for(int p=0;p<8;++p) r+=red[p*32+t]; o[s*448+h*28+t]=__float2bfloat16(r); }
}
extern "C" void kernel_launch(void* const* d_in, const int* in_sizes, int n_in, void* d_out, int out_size, void* d_ws, size_t ws_size, hipStream_t stream) {
  const bf16* Ax=(const bf16*)d_in[0]; const bf16* Aw=(const bf16*)d_in[1]; const bf16* Ao=(const bf16*)d_in[2];
  const bf16* Bx=(const bf16*)d_in[3]; const bf16* Bw=(const bf16*)d_in[4]; const bf16* Bo=(const bf16*)d_in[5];
  bf16* out=(bf16*)d_out; bf16* ws=(bf16*)d_ws;
  bf16* qkvA=ws; bf16* qkvB=ws+2048*1344; bf16* oA=qkvB+2048*1344; bf16* oB=oA+2048*448;
  gemm_naive<<<dim3(21,512),dim3(64,4),0,stream>>>(Ax,Aw,qkvA,2048,1344,448,1344);
  gemm_naive<<<dim3(21,512),dim3(64,4),0,stream>>>(Bx,Bw,qkvB,2048,1344,448,1344);
  local_attn<<<512,dim3(16,4),0,stream>>>(qkvA,oA);
  full_attn<<<dim3(2048,16),256,0,stream>>>(qkvB,oB);
  gemm_naive<<<dim3(7,512),dim3(64,4),0,stream>>>(oA,Ao,out,2048,448,448,896);
  gemm_naive<<<dim3(7,512),dim3(64,4),0,stream>>>(oB,Bo,out+448,2048,448,448,896);
}
```

```cpp
#include <hip/hip_runtime.h>
#include <hip/hip_bf16.h>

typedef unsigned u32;
typedef unsigned long long u64;
typedef short bf16x8 __attribute__((ext_vector_type(8)));
typedef float f32x16 __attribute__((ext_vector_type(16)));
typedef float f32x4 __attribute__((ext_vector_type(4)));
typedef float f32x2 __attribute__((ext_vector_type(2)));
typedef u32 u32x4 __attribute__((ext_vector_type(4)));
typedef u32 u32x2 __attribute__((ext_vector_type(2)));
typedef __bf16 bf2_t __attribute__((ext_vector_type(2)));
#define LAS __attribute__((address_space(3)))
#define GAS __attribute__((address_space(1)))
#define DI __device__ __forceinline__
#define SBAR() __builtin_amdgcn_sched_barrier(0)
#define PIN(x) asm volatile("" : "+v"(x))
#define MFMA(a, b, c) __builtin_amdgcn_mfma_f32_32x32x16_bf16(__builtin_bit_cast(bf16x8, a), __builtin_bit_cast(bf16x8, b), (c), 0, 0, 0)
#define RLX_AGENT __ATOMIC_RELAXED, __HIP_MEMORY_SCOPE_AGENT

constexpr u32 WS_GEN = 0;
constexpr u32 WS_FST = 2048;
constexpr u32 WS_FKV = 4096;
constexpr u32 WS_FO = WS_FKV + 8192;
constexpr u32 WS_KV = 65536;
constexpr u32 WS_Q = WS_KV + 4194304;
constexpr u32 WS_O = WS_Q + 2097152;
constexpr u32 WS_END = WS_O + 2 * 2097152;
constexpr u32 WS_STAMP = WS_END;

constexpr u32 SLOT_B = 39936, SLOT_HALO = 16384, SLOT_W = 18432;
constexpr u32 L_WO = 0, WO_B = 50176;
constexpr u32 L_SCR = 2 * WO_B;
constexpr u32 L_LP = L_SCR + 57344;
constexpr u32 LDS_BYTES = 163840;

constexpr float SCALE = 0.18898223650461362f;
constexpr float LOG2E = 1.4426950408889634f;
constexpr float QSC = SCALE * LOG2E;
constexpr float MSHIFT = 40.0f;
constexpr u32 SPIN_CAP = 1u << 17;

DI u32 pk(float lo, float hi) { f32x2 v = {lo, hi}; return __builtin_bit_cast(u32, __builtin_convertvector(v, bf2_t)); }
DI u32x4 pk8(const f32x16& a, int b) { u32x4 r = {pk(a[b], a[b + 1]), pk(a[b + 2], a[b + 3]), pk(a[b + 4], a[b + 5]), pk(a[b + 6], a[b + 7])}; return r; }
DI float blo(u32 x) { return __uint_as_float(x << 16); }
DI float bhi(u32 x) { return __uint_as_float(x & 0xffff0000u); }
DI float sel(bool c, float a, float b) { PIN(a); PIN(b); return c ? a : b; }
DI float swapmax(float m) { auto r = __builtin_amdgcn_permlane32_swap(__float_as_uint(m), __float_as_uint(m), false, false); return fmaxf(__uint_as_float(r[0]), __uint_as_float(r[1])); }
DI float swapadd(float m) { auto r = __builtin_amdgcn_permlane32_swap(__float_as_uint(m), __float_as_uint(m), false, false); return __uint_as_float(r[0]) + __uint_as_float(r[1]); }
DI void glds16(u32 voff, const void* sbase, u32 ldsaddr) {
  u32 keep;
  asm volatile("s_mov_b32 %0, m0\n\ts_mov_b32 m0, %3\n\ts_nop 0\n\tglobal_load_lds_dwordx4 %1, %2\n\ts_mov_b32 m0, %0"
               : "=&s"(keep) : "v"(voff), "s"(sbase), "s"(ldsaddr) : "memory");
}
DI void glds16c(u32 a, u32 b, u32 voff, const void* sbase, u32 ldsaddr) {
  u32 keep;
  asm volatile("s_cmp_lt_u32 %4, %5\n\ts_cbranch_scc0 1f\n\ts_mov_b32 %0, m0\n\ts_mov_b32 m0, %3\n\ts_nop 0\n\tglobal_load_lds_dwordx4 %1, %2\n\ts_mov_b32 m0, %0\n1:"
               : "=&s"(keep) : "v"(voff), "s"(sbase), "s"(ldsaddr), "s"(a), "s"(b) : "memory", "scc");
}
DI void glds63c(u32 a, u32 b, u32 voff, const void* sbase, u32 ldsaddr) {
  u32 keep;
  asm volatile("s_cmp_lt_u32 %4, %5\n\ts_cbranch_scc0 1f\n\ts_mov_b32 %0, m0\n\ts_mov_b32 m0, %3\n\ts_bitset0_b32 exec_hi, 31\n\tglobal_load_lds_dwordx4 %1, %2\n\ts_bitset1_b32 exec_hi, 31\n\ts_mov_b32 m0, %0\n1:"
               : "=&s"(keep) : "v"(voff), "s"(sbase), "s"(ldsaddr), "s"(a), "s"(b) : "memory", "scc");
}
DI u32x4 ldsr128(const LAS char* p) { return *(const LAS u32x4*)p; }
DI void ldsw128(LAS char* p, u32x4 v) { *(LAS u32x4*)p = v; }
typedef short v4i16_t __attribute__((ext_vector_type(4)));
DI u32x2 ldstr(const LAS char* p) { return __builtin_bit_cast(u32x2, __builtin_amdgcn_ds_read_tr16_b64_v4i16((LAS v4i16_t*)p)); }
DI u32x4 trfrag(const LAS char* p0, const LAS char* p1) { u32x2 a = ldstr(p0), b = ldstr(p1); u32x4 r = {a[0], a[1], b[0], b[1]}; return r; }
#define WAIT_BAR(N) asm volatile("s_waitcnt vmcnt(" #N ") lgkmcnt(0)\n\ts_barrier" ::: "memory")
#define LBAR() do { asm volatile("s_waitcnt lgkmcnt(0)\n\ts_barrier" ::: "memory"); SBAR(); } while (0)

DI void wait_flags(const char* ws, u32 laneoff, u64 tag) {
  const GAS u64* f = (const GAS u64*)(ws + laneoff);
#pragma unroll 1
  for (u32 spins = 0; spins < SPIN_CAP; ++spins) {
    u64 v = __hip_atomic_load(f, RLX_AGENT);
    if (__all(v == tag)) break;
    __builtin_amdgcn_s_sleep(1);
  }
  __builtin_amdgcn_fence(__ATOMIC_ACQUIRE, "wavefront");
  asm volatile("" ::: "memory");
}
DI void set_flag(char* ws, u32 off, u64 tag) { __hip_atomic_store((GAS u64*)(ws + off), tag, RLX_AGENT); }

#define ST(i) do {} while (0)

struct Frags { u32x4 x[2], q[2], k[2], v[2], hx[2]; };

template <int HALO>
DI void qkv_gemm(LAS char* lds, u32 lds0, const char* xsrc, const char* wsrc, u32 has5, u32 off5, int rot, int w, u32 voffX, u32 voffW0, u32 voffW1, u32 voff5,
                 u32 xa0, u32 xh0, u32 wa0, f32x16& aq, f32x16& ak, f32x16& av, f32x16& ah, char* ws, u64 tag, bool first) {
  Frags F0, F1;
  auto rd = [&](Frags& F, int c, int hc) {
    const u32 sb = (u32)((c + 1) & 3) * SLOT_B;
    const u32 xa = sb + xa0; const LAS char* wp = lds + sb + wa0 + hc * (32 * 336);
#pragma unroll
    for (int j = 0; j < 2; ++j) {
      F.x[j] = ldsr128(lds + (xa ^ ((2 * hc + j) << 5)));
      F.q[j] = trfrag(wp + j * 5376, wp + j * 5376 + 336);
      F.k[j] = trfrag(wp + j * 5376 + 112, wp + j * 5376 + 336 + 112);
      F.v[j] = trfrag(wp + j * 5376 + 224, wp + j * 5376 + 336 + 224);
      if (HALO) F.hx[j] = ldsr128(lds + ((sb + xh0) ^ ((2 * hc + j) << 5)));
    }
  };
  auto mm = [&](Frags& F, int j) {
    aq = MFMA(F.q[j], F.x[j], aq); ak = MFMA(F.k[j], F.x[j], ak); av = MFMA(F.x[j], F.v[j], av);
    if (HALO == 1) ah = MFMA(F.k[j], F.hx[j], ah);
    if (HALO == 2) ah = MFMA(F.hx[j], F.v[j], ah);
  };
  asm volatile("s_cmp_lt_u32 %0, 5\n\ts_cbranch_scc1 1f\n\ts_waitcnt vmcnt(4)\n1:\n\ts_waitcnt vmcnt(5)\n\ts_barrier" :: "s"(w) : "memory", "scc");
  if (first) set_flag(ws, WS_FST + blockIdx.x * 8, tag);
  rd(F0, 0, 0);
  int kc = rot + 2; kc = kc >= 7 ? kc - 7 : kc;
#pragma unroll 1
  for (int c = 0; c < 7; ++c) {
    SBAR();
    rd(F1, c, 1);
    SBAR();
    const u32 sb = lds0 + (u32)((c + 3) & 3) * SLOT_B + w * 1024;
    const char* xs = xsrc + kc * 128; const char* wq = wsrc + kc * (64 * 2688);
    kc = kc == 6 ? 0 : kc + 1;
    aq = MFMA(F0.q[0], F0.x[0], aq); SBAR(); glds16c(c, 5, voffX, xs, sb); SBAR();
    ak = MFMA(F0.k[0], F0.x[0], ak); SBAR(); glds16c(c, 5, voffW0, wq, sb + SLOT_W); SBAR();
    av = MFMA(F0.x[0], F0.v[0], av); SBAR(); glds16c(c, 5, voffX, xs + 64 * 896, sb + 8192); SBAR();
    aq = MFMA(F0.q[1], F0.x[1], aq); SBAR(); glds16c(c, 5, voffW1, wq, sb + SLOT_W + 8192); SBAR();
    ak = MFMA(F0.k[1], F0.x[1], ak); SBAR(); glds16c(c, has5, voff5, w < 5 ? wq : xs, sb + off5); SBAR();
    av = MFMA(F0.x[1], F0.v[1], av);
    if (HALO == 1) { ah = MFMA(F0.k[0], F0.hx[0], ah); ah = MFMA(F0.k[1], F0.hx[1], ah); }
    if (HALO == 2) { ah = MFMA(F0.hx[0], F0.v[0], ah); ah = MFMA(F0.hx[1], F0.v[1], ah); }
    SBAR();
    __builtin_amdgcn_s_waitcnt(0xC07F);
    asm volatile("s_cmp_lt_u32 %0, 5\n\ts_cbranch_scc1 1f\n\ts_waitcnt vmcnt(0)\n"
                 "1:\n\ts_cmp_lt_u32 %1, 5\n\ts_cbranch_scc1 2f\n\ts_waitcnt vmcnt(4)\n2:\n\ts_waitcnt vmcnt(5)\n\ts_barrier" :: "s"(c), "s"(w) : "memory", "scc");
    SBAR();
    rd(F0, c + 1, 0);
    SBAR();
    mm(F1, 0); mm(F1, 1);
  }
}

__global__ __launch_bounds__(512, 2) void fused_attn(const char* __restrict__ Ax, const char* __restrict__ Aw, const char* __restrict__ Ao,
                                                     const char* __restrict__ Bx, const char* __restrict__ Bw, const char* __restrict__ Bo,
                                                     char* __restrict__ out, char* ws) {
  __shared__ __attribute__((aligned(16))) char lds_raw[LDS_BYTES];
  LAS char* lds = (LAS char*)lds_raw;
  const u32 lds0 = (u32)(size_t)lds;
  const int tid = threadIdx.x, lane = tid & 63, r32 = lane & 31, hh = lane >> 5;
  const int w = __builtin_amdgcn_readfirstlane(tid >> 6);
  const int bid = blockIdx.x, xcd = bid & 7, slot = bid >> 3;
  const bool isA = xcd >= 4;
  const int rb = 8 * (xcd & 1) + (slot & 7), hp = 4 * ((xcd >> 1) & 1) + (slot >> 3);
  const int h = 2 * xcd + (slot >> 4), qb = slot & 15;
  const int cg = slot & 7, ot0 = 4 * h + 2 * ((slot >> 3) & 1);
  const int tq = w & 3, hf = w >> 2;
  ST(0);
  const char* xsrc = isA ? Ax : Bx; const char* wsrc = isA ? Aw : Bw;
  const u32 has5 = (w < 5 || (isA && w < 7)) ? 5u : 0u, off5 = w < 5 ? SLOT_W + 16384 : SLOT_HALO - 5 * 1024;

  const int prow = 8 * w + (lane >> 3);
  const u32 voffX = (u32)(128 * rb + prow) * 896u + (u32)(((lane & 7) ^ ((prow >> 1) & 7)) << 4);
  auto wsrc_off = [&](int p) { const int i = 64 * p + lane, ro = i / 21, c = i - ro * 21, t = c / 7, pc = c - t * 7;
    const int kr = (ro & ~15) | ((ro & 3) << 2) | ((ro >> 2) & 3); return (u32)(kr * 2688 + t * 896 + hp * 112 + pc * 16); };
  const u32 voffW0 = wsrc_off(w), voffW1 = wsrc_off(w + 8);
  u32 voff5;
  if (w < 5) voff5 = wsrc_off(w + 16);
  else { const int hr = 8 * ((w - 5) & 1) + (lane >> 3); int tok = 128 * rb - 16 + hr; tok = tok < 0 ? 0 : tok; voff5 = (u32)tok * 896u + (u32)(((lane & 7) ^ ((hr >> 1) & 7)) << 4); }
  const int rot = ((slot & 7) + 2 * (slot >> 3)) % 7;
#pragma unroll
  for (int c = 0; c < 2; ++c) {
    const u32 sb = lds0 + (u32)((c + 1) & 3) * SLOT_B + w * 1024;
    const int kc = (rot + c == 7) ? 0 : rot + c;
    const char* xs = xsrc + kc * 128; const char* wq = wsrc + kc * (64 * 2688);
    glds16(voffX, xs, sb); glds16(voffW0, wq, sb + SLOT_W); glds16(voffX, xs + 64 * 896, sb + 8192); glds16(voffW1, wq, sb + SLOT_W + 8192);
    glds16c(0, has5, voff5, w < 5 ? wq : xs, sb + off5);
  }
  const __amdgpu_buffer_rsrc_t rs = __builtin_amdgcn_make_buffer_rsrc(ws, 0, WS_END, 0x00020000);
  u64 gen = *(const __attribute__((address_space(4))) u64*)(ws + WS_GEN);
  u64 tag = gen * 6364136223846793005ull + 1442695040888963407ull;
  asm volatile("" : "+s"(tag));

  const f32x16 zero16 = {};
  f32x16 aq = zero16, ak = zero16, av = zero16, ah = zero16;
  const int hrole = (isA && (tq >> 1) == hf) ? 1 + (tq & 1) : 0;
  {
    const int xrow = 32 * tq + r32;
    const u32 xa0 = xrow * 128 + ((hh ^ ((xrow >> 1) & 7)) << 4);
    const u32 xh0 = SLOT_HALO + (r32 & 15) * 128 + ((hh ^ (((r32 & 15) >> 1) & 7)) << 4);
    const u32 wa0 = SLOT_W + (4 * ((lane & 15) >> 2) + 2 * hh) * 336 + hf * 56 + ((lane >> 4) & 1) * 32 + (lane & 3) * 8;
    if (hrole == 0) qkv_gemm<0>(lds, lds0, xsrc, wsrc, has5, off5, rot, w, voffX, voffW0, voffW1, voff5, xa0, xh0, wa0, aq, ak, av, ah, ws, tag, tid == 0);
    else if (hrole == 1) qkv_gemm<1>(lds, lds0, xsrc, wsrc, has5, off5, rot, w, voffX, voffW0, voffW1, voff5, xa0, xh0, wa0, aq, ak, av, ah, ws, tag, tid == 0);
    else qkv_gemm<2>(lds, lds0, xsrc, wsrc, has5, off5, rot, w, voffX, voffW0, voffW1, voff5, xa0, xh0, wa0, aq, ak, av, ah, ws, tag, tid == 0);
  }
  ST(1);
  const int ph = 2 * hp + hf, ptile = 4 * rb + tq;
  if (!isA) {
    const u32 kvo = WS_KV + (u32)((ph * 64 + ptile) * 4) * 1024 + lane * 16;
    u32x4 k0 = pk8(ak, 0), k1 = pk8(ak, 8);
    if (hh) { k1[2] = 0x00003F80u; k1[3] = 0; }
    __builtin_amdgcn_raw_buffer_store_b128(k0, rs, kvo, 0, 16);
    __builtin_amdgcn_raw_buffer_store_b128(k1, rs, kvo + 1024, 0, 16);
    aq *= QSC;
    u32x4 q0 = pk8(aq, 0), q1 = pk8(aq, 8);
    if (hh) { q1[2] = 0; q1[3] = 0; }
    f32x16 s = MFMA(k0, q0, zero16); s = MFMA(k1, q1, s);
    u32x4 v0 = pk8(av, 0), v1 = pk8(av, 8);
    if (r32 >= 28) { const u32 f = r32 == 28 ? 0x3F803F80u : 0u; v0 = (u32x4){f, f, f, f}; v1 = v0; }
    __builtin_amdgcn_raw_buffer_store_b128(v0, rs, kvo + 2048, 0, 16);
    __builtin_amdgcn_raw_buffer_store_b128(v1, rs, kvo + 3072, 0, 16);
    const u32 qo = WS_Q + (u32)((ph * 64 + ptile) * 2) * 1024 + lane * 16;
    __builtin_amdgcn_raw_buffer_store_b128(q0, rs, qo, 0, 16);
    float m = s[0];
#pragma unroll
    for (int i = 1; i < 16; ++i) m = fmaxf(m, s[i]);
    m = swapmax(m);
    if (hh) q1[2] = pk(-(m + MSHIFT), 0.f);
    __builtin_amdgcn_raw_buffer_store_b128(q1, rs, qo + 1024, 0, 16);
    asm volatile("s_waitcnt vmcnt(0)" ::: "memory");
    if (lane == 0) set_flag(ws, WS_FKV + (u32)(ph * 64 + ptile) * 8, tag);
    if (bid == 0 && w == 7) {
#pragma unroll 1
      for (int j = 0; j < 4; ++j) wait_flags(ws, WS_FST + (u32)(j * 64 + lane) * 8, tag);
      if (lane == 0) *(GAS u64*)(ws + WS_GEN) = tag;
    }
  } else {
    LAS char* xb = lds + hf * 12288;
    u32x4 aq0 = pk8(aq, 0), aq1 = pk8(aq, 8), ak0 = pk8(ak, 0), ak1 = pk8(ak, 8), cv0 = pk8(av, 0), cv1 = pk8(av, 8);
    if (hh) { aq1[2] = 0; aq1[3] = 0; ak1[2] = 0; ak1[3] = 0; }
    if (r32 >= 28) { cv0 = (u32x4){0, 0, 0, 0}; cv1 = cv0; }
    if (tq < 3) { LAS char* p = xb + (tq + 1) * 3072 + lane * 16; ldsw128(p, ak0); ldsw128(p + 1024, ak1); ldsw128(p + 2048, cv1); }
    if (hrole == 1) {
      u32x4 h0 = pk8(ah, 0), h1 = pk8(ah, 8);
      if (hh) { h1[2] = 0; h1[3] = 0; }
      ldsw128(xb + lane * 16, h0); ldsw128(xb + 1024 + lane * 16, h1);
    } else if (hrole == 2) {
      u32x4 h1 = pk8(ah, 8);
      if (r32 >= 28) h1 = (u32x4){0, 0, 0, 0};
      ldsw128(xb + 2048 + lane * 16, h1);
    }
    f32x16 sc = MFMA(ak0, aq0, zero16); sc = MFMA(ak1, aq1, sc);
    LBAR();
    u32x4 pk0 = ldsr128(xb + tq * 3072 + lane * 16), pk1 = ldsr128(xb + tq * 3072 + 1024 + lane * 16), pv1 = ldsr128(xb + tq * 3072 + 2048 + lane * 16);
    f32x16 sp = MFMA(pk0, aq0, zero16); sp = MFMA(pk1, aq1, sp);
    const bool odd = r32 & 1; const bool pvalid = ptile != 0;
    float t[12];
    float mx = -INFINITY;
#pragma unroll
    for (int i = 0; i < 12; ++i) {
      const int rp = i < 8 ? 2 * i : 2 * i - 8;
      const float sv = i < 8 ? sel(odd, sc[rp + 1], sc[rp]) : sel(odd, sp[rp + 1], sp[rp]);
      const int rho = (rp & 3) + 8 * (rp >> 2) + 4 * hh;
      const int dl = (r32 & ~1) - rho + (i < 8 ? 0 : 32);
      const bool ok = (unsigned)dl <= 14u && (i < 8 || pvalid);
      const float l1 = blo(pk(sv, 0.f)) * SCALE;
      const float l2 = blo(pk(l1, 0.f));
      t[i] = ok ? l2 : -INFINITY;
      mx = fmaxf(mx, t[i]);
    }
    mx = swapmax(mx);
    float sum = 0.f;
#pragma unroll
    for (int i = 0; i < 12; ++i) { t[i] = __builtin_amdgcn_exp2f((t[i] - mx) * LOG2E); sum += t[i]; }
    sum = swapadd(sum);
    const float inv = __builtin_amdgcn_rcpf(sum);
    u32 pw[12];
#pragma unroll
    for (int i = 0; i < 12; ++i) { const u32 b = pk(t[i] * inv, 0.f); pw[i] = odd ? (b << 16) : b; }
    const u32x4 p0 = {pw[0], pw[1], pw[2], pw[3]}, p1 = {pw[4], pw[5], pw[6], pw[7]}, pp = {pw[8], pw[9], pw[10], pw[11]};
    f32x16 o = MFMA(cv0, p0, zero16); o = MFMA(cv1, p1, o); o = MFMA(pv1, pp, o);
    const u32 oi = (u32)((ptile * 16 + ph) * 2);
    __builtin_amdgcn_raw_buffer_store_b128(pk8(o, 0), rs, WS_O + oi * 1024 + lane * 16, 0, 16);
    __builtin_amdgcn_raw_buffer_store_b128(pk8(o, 8), rs, WS_O + oi * 1024 + 1024 + lane * 16, 0, 16);
  }
  ST(2);
  wait_flags(ws, WS_FKV + (u32)(h * 64 + ((lane & 8) ? 4 * qb + (w >> 1) : 8 * w + (lane & 7))) * 8, tag);
  if (isA) {
    asm volatile("s_waitcnt vmcnt(0)" ::: "memory");
    if (lane < 2) set_flag(ws, WS_FO + (u32)((ptile * 16 + ph) * 2 + lane) * 8, tag);
  }
  ST(3);
  u32x4 qf[4][2];
  f32x16 o[4];
  {
    const u32 kvb = WS_KV + (u32)((h * 64 + 8 * w) * 4) * 1024 + lane * 16;
    u32x4 kv[8][4];
    const int trot = qb >> 1;
    const u32 voffO = (u32)((lane / 7) * 896 + (lane % 7) * 16 + cg * 112);
#define LOADF(t, f) kv[t][f] = __builtin_amdgcn_raw_buffer_load_b128(rs, kvb + (f) * 1024, (((t) + trot) & 7) * 4096, 16)
#define LOADT(t) do { LOADF(t, 0); LOADF(t, 1); LOADF(t, 2); LOADF(t, 3); } while (0)
#define WOP(j) do { const int p_ = w + 8 * (j), q_ = p_ >= 50 ? p_ - 50 : p_, r_ = q_ == 49 ? 439 : 9 * q_; \
      glds63c(p_, 100, voffO, (p_ >= 50 ? Bo : Ao) + r_ * 896, lds0 + (p_ >= 50 ? WO_B : 0) + r_ * 112); } while (0)
    const u32x4 qmine = __builtin_amdgcn_raw_buffer_load_b128(rs, WS_Q + (u32)((h * 64 + 4 * qb) * 2 + w) * 1024 + lane * 16, 0, 16);
    LOADT(0); LOADT(1);
    SBAR();
    ldsw128(lds + L_SCR + w * 1024 + lane * 16, qmine);
    LBAR();
#pragma unroll
    for (int i = 0; i < 4; ++i) {
      const u32 a = L_SCR + (u32)((i ^ (w >> 1)) * 2048) + lane * 16;
      qf[i][0] = ldsr128(lds + a); qf[i][1] = ldsr128(lds + a + 1024);
    }
    LOADT(2);
    LBAR();
    ST(4);
#pragma unroll
    for (int i = 0; i < 4; ++i) o[i] = zero16;
    f32x16 s[2]; u32x4 pa, pb;
    s[0] = MFMA(kv[0][0], qf[0][0], zero16); s[0] = MFMA(kv[0][1], qf[0][1], s[0]);
    SBAR();
#define EX4(S, b) do { S[b] = __builtin_amdgcn_exp2f(S[b]); S[b + 1] = __builtin_amdgcn_exp2f(S[b + 1]); S[b + 2] = __builtin_amdgcn_exp2f(S[b + 2]); S[b + 3] = __builtin_amdgcn_exp2f(S[b + 3]); } while (0)
#define STEP(u) do { \
      constexpr int kt = (u) >> 2, qt = (u) & 3, un = (u) + 1, ktn = (un >> 2) & 7, qtn = un & 3, up = (u) > 0 ? (u) - 1 : 0, kl = (kt + 3) & 7; \
      f32x16& sc = s[(u) & 1]; f32x16& sn = s[un & 1]; \
      if (un < 32) { sn = MFMA(kv[ktn][0], qf[qtn][0], zero16); PIN(sn); } SBAR(); \
      if (kt + 3 < 8) LOADF(kl, qt); \
      if ((u) >= 4 && (u) < 30 && ((u) & 1) == 0) WOP(((u) - 4) >> 1); \
      EX4(sc, 0); PIN(sc); SBAR(); \
      if (un < 32) { sn = MFMA(kv[ktn][1], qf[qtn][1], sn); PIN(sn); } SBAR(); \
      EX4(sc, 4); pa[0] = pk(sc[0], sc[1]); pa[1] = pk(sc[2], sc[3]); PIN(sc); PIN(pa); SBAR(); \
      if ((u) > 0) { o[up & 3] = MFMA(kv[up >> 2][3], pb, o[up & 3]); PIN(o[up & 3]); } SBAR(); \
      EX4(sc, 8); pa[2] = pk(sc[4], sc[5]); pa[3] = pk(sc[6], sc[7]); PIN(pa); sc[12] = __builtin_amdgcn_exp2f(sc[12]); PIN(sc); SBAR(); \
      o[qt] = MFMA(kv[kt][2], pa, o[qt]); PIN(o[qt]); SBAR(); \
      sc[13] = __builtin_amdgcn_exp2f(sc[13]); sc[14] = __builtin_amdgcn_exp2f(sc[14]); sc[15] = __builtin_amdgcn_exp2f(sc[15]); \
      pb[0] = pk(sc[8], sc[9]); pb[1] = pk(sc[10], sc[11]); pb[2] = pk(sc[12], sc[13]); pb[3] = pk(sc[14], sc[15]); PIN(pb); SBAR(); \
    } while (0)
#define STEP4(t) STEP(4 * (t)); STEP(4 * (t) + 1); STEP(4 * (t) + 2); STEP(4 * (t) + 3)
    STEP4(0); STEP4(1); STEP4(2); STEP4(3); STEP4(4); STEP4(5); STEP4(6); STEP4(7);
    o[3] = MFMA(kv[7][3], pb, o[3]);
  }
  ST(5);
  const int qt2 = w & 1, kq = w >> 1, otile = ot0 + qt2;
  const u32 ofi = (u32)((otile * 16 + 4 * kq) * 2);
  u32x4 of[8];
  wait_flags(ws, WS_FO + (ofi + (lane & 7)) * 8, tag);
#pragma unroll
  for (int i = 0; i < 8; ++i) of[i] = __builtin_amdgcn_raw_buffer_load_b128(rs, WS_O + ofi * 1024 + lane * 16, i * 1024, 16);

  {
    const int ws1 = w & 1;
    float own[8];
#pragma unroll
    for (int j = 0; j < 8; ++j) own[j] = sel(ws1, o[0][8 + j], o[0][j]);
    { const u32x4 a = pk8(o[0], 0), b = pk8(o[0], 8); const u32x4 oth = ws1 ? a : b;
      ldsw128(lds + L_SCR + (u32)((w ^ 1) * 7 + 0) * 1024 + lane * 16, oth); }
#pragma unroll
    for (int i = 1; i < 4; ++i)
#pragma unroll
      for (int s2 = 0; s2 < 2; ++s2) {
        const int owner = ((i ^ (w >> 1)) << 1) | s2;
        ldsw128(lds + L_SCR + (u32)(owner * 7 + (w ^ owner) - 1) * 1024 + lane * 16, pk8(o[i], 8 * s2));
      }
    if (hh) {
#pragma unroll
      for (int i = 0; i < 4; ++i) *(LAS float*)(lds + L_LP + (u32)(((i ^ (w >> 1)) * 32 + r32) * 32 + w * 4)) = o[i][12];
    }
    LBAR();
    ST(6);
    const f32x4 la = *(const LAS f32x4*)(lds + L_LP + (u32)(((w >> 1) * 32 + r32) * 32)), lb = *(const LAS f32x4*)(lds + L_LP + (u32)(((w >> 1) * 32 + r32) * 32 + 16));
    float l = ((la[0] + la[1]) + (la[2] + la[3])) + ((lb[0] + lb[1]) + (lb[2] + lb[3]));
#pragma unroll
    for (int k = 0; k < 7; ++k) {
      const u32x4 v = ldsr128(lds + L_SCR + (u32)(w * 7 + k) * 1024 + lane * 16);
#pragma unroll
      for (int j = 0; j < 4; ++j) { own[2 * j] += blo(v[j]); own[2 * j + 1] += bhi(v[j]); }
    }
    float chk = 0.f;
#pragma unroll
    for (int j = 0; j < 8; ++j) chk = __builtin_fmaf(own[j], 0.f, chk);
    const bool good = (l >= 0x1p-70f) && (l <= 0x1p126f) && (chk == 0.f);
    if (__builtin_expect(!__all(good), 0)) {
      wait_flags(ws, WS_FKV + (u32)(h * 64 + lane) * 8, tag);
      u32x4 q0 = qf[0][0], q1 = qf[0][1];
      if (hh) q1[2] = 0;
      const u32 kb = WS_KV + (u32)(h * 64 * 4) * 1024 + lane * 16;
      float m = -INFINITY;
#pragma unroll 1
      for (int t = 0; t < 64; ++t) {
        const u32x4 k0 = __builtin_amdgcn_raw_buffer_load_b128(rs, kb, t * 4096, 16), k1 = __builtin_amdgcn_raw_buffer_load_b128(rs, kb + 1024, t * 4096, 16);
        f32x16 s = MFMA(k0, q0, zero16); s = MFMA(k1, q1, s);
#pragma unroll
        for (int i = 0; i < 16; ++i) m = fmaxf(m, s[i]);
      }
      m = swapmax(m);
      f32x16 oo = zero16;
#pragma unroll 1
      for (int t = 0; t < 64; ++t) {
        const u32x4 k0 = __builtin_amdgcn_raw_buffer_load_b128(rs, kb, t * 4096, 16), k1 = __builtin_amdgcn_raw_buffer_load_b128(rs, kb + 1024, t * 4096, 16);
        const u32x4 v0 = __builtin_amdgcn_raw_buffer_load_b128(rs, kb + 2048, t * 4096, 16), v1 = __builtin_amdgcn_raw_buffer_load_b128(rs, kb + 3072, t * 4096, 16);
        f32x16 s = MFMA(k0, q0, zero16); s = MFMA(k1, q1, s);
#pragma unroll
        for (int i = 0; i < 16; ++i) s[i] = __builtin_amdgcn_exp2f(s[i] - m);
        oo = MFMA(v0, pk8(s, 0), oo); oo = MFMA(v1, pk8(s, 8), oo);
      }
      { auto r = __builtin_amdgcn_permlane32_swap(__float_as_uint(oo[12]), __float_as_uint(oo[12]), false, false); l = __uint_as_float(r[1]); }
#pragma unroll
      for (int j = 0; j < 8; ++j) own[j] = sel(ws1, oo[8 + j], oo[j]);
    }
    const float inv = __builtin_amdgcn_rcpf(l);
    u32x4 ob = {pk(own[0] * inv, own[1] * inv), pk(own[2] * inv, own[3] * inv), pk(own[4] * inv, own[5] * inv), pk(own[6] * inv, own[7] * inv)};
    if (ws1 && hh) { ob[2] = 0; ob[3] = 0; }
    const u32 oi = (u32)(((4 * qb + (w >> 1)) * 16 + h) * 2 + ws1);
    __builtin_amdgcn_raw_buffer_store_b128(ob, rs, WS_O + 2097152 + oi * 1024 + lane * 16, 0, 16);
    asm volatile("s_waitcnt vmcnt(0)" ::: "memory");
    if (lane == 0) set_flag(ws, WS_FO + 16384 + oi * 8, tag);
  }
  ST(7);
  const u32 wob = (u32)(kq * 4 * 3136 + ((lane & 15) >> 2) * 112 + ((((lane >> 4) & 1) ^ (kq & 1)) * 32) + (lane & 3) * 8);
  const u32 nt0 = (u32)(kq >> 1) * 56, nt1 = 56 - nt0;
#pragma unroll 1
  for (int br = 0; br < 2; ++br) {
    if (br == 1) {
      wait_flags(ws, WS_FO + 16384 + (ofi + (lane & 7)) * 8, tag);
#pragma unroll
      for (int i = 0; i < 8; ++i) of[i] = __builtin_amdgcn_raw_buffer_load_b128(rs, WS_O + 2097152 + ofi * 1024 + lane * 16, i * 1024, 16);
      ST(9);
    }
    const LAS char* wb = lds + L_WO + br * WO_B + wob;
    const LAS char* wa = wb + hh * 448;
    f32x16 c0 = zero16, c1 = zero16;
#pragma unroll
    for (int i = 0; i < 4; ++i) {
      const u32x4 f00 = trfrag(wa + nt0 + i * 3136, wa + nt0 + i * 3136 + 896);
      const u32x4 f01 = trfrag(wa + nt0 + i * 3136 + 1792, wb + nt0 + i * 3136 + 2688);
      const u32x4 f10 = trfrag(wa + nt1 + i * 3136, wa + nt1 + i * 3136 + 896);
      const u32x4 f11 = trfrag(wa + nt1 + i * 3136 + 1792, wb + nt1 + i * 3136 + 2688);
      c0 = MFMA(f00, of[2 * i], c0); c1 = MFMA(f10, of[2 * i], c1);
      c0 = MFMA(f01, of[2 * i + 1], c0); c1 = MFMA(f11, of[2 * i + 1], c1);
    }
    LBAR();
    LAS char* rb2 = lds + L_SCR + lane * 16;
#pragma unroll
    for (int sg = 1; sg < 4; ++sg) {
      const f32x16& c = sg < 2 ? c0 : c1; const int b = 8 * (sg & 1);
      LAS char* p = rb2 + (u32)(((qt2 * 4 + (kq ^ sg)) * 3 + sg - 1) * 2048);
      *(LAS f32x4*)p = (f32x4){c[b], c[b + 1], c[b + 2], c[b + 3]};
      *(LAS f32x4*)(p + 1024) = (f32x4){c[b + 4], c[b + 5], c[b + 6], c[b + 7]};
    }
    LBAR();
    f32x4 ra = {c0[0], c0[1], c0[2], c0[3]}, rc = {c0[4], c0[5], c0[6], c0[7]};
#pragma unroll
    for (int k = 0; k < 3; ++k) {
      const LAS char* p = rb2 + (u32)(((qt2 * 4 + kq) * 3 + k) * 2048);
      ra += *(const LAS f32x4*)p; rc += *(const LAS f32x4*)(p + 1024);
    }
    char* op = out + (size_t)(32 * otile + r32) * 1792 + br * 896 + cg * 112 + nt0 + (kq & 1) * 32 + hh * 8;
    *(u32x2*)op = (u32x2){pk(ra[0], ra[1]), pk(ra[2], ra[3])};
    if (!((kq & 1) && hh)) *(u32x2*)(op + 16) = (u32x2){pk(rc[0], rc[1]), pk(rc[2], rc[3])};
    ST(8 + 2 * br);
  }
}

extern "C" void kernel_launch(void* const* d_in, const int* in_sizes, int n_in, void* d_out, int out_size, void* d_ws, size_t ws_size,
                              hipStream_t stream) {
  fused_attn<<<256, 512, 0, stream>>>((const char*)d_in[0], (const char*)d_in[1], (const char*)d_in[2], (const char*)d_in[3],
                                      (const char*)d_in[4], (const char*)d_in[5], (char*)d_out, (char*)d_ws);
}
```

```cpp
#include <hip/hip_runtime.h>
#include <hip/hip_bf16.h>

typedef unsigned u32;
typedef unsigned long long u64;
typedef short bf16x8 __attribute__((ext_vector_type(8)));
typedef float f32x16 __attribute__((ext_vector_type(16)));
typedef float f32x4 __attribute__((ext_vector_type(4)));
typedef float f32x2 __attribute__((ext_vector_type(2)));
typedef u32 u32x4 __attribute__((ext_vector_type(4)));
typedef u32 u32x2 __attribute__((ext_vector_type(2)));
typedef __bf16 bf2_t __attribute__((ext_vector_type(2)));
#define LAS __attribute__((address_space(3)))
#define GAS __attribute__((address_space(1)))
#define DI __device__ __forceinline__
#define SBAR() __builtin_amdgcn_sched_barrier(0)
#define PIN(x) asm volatile("" : "+v"(x))
#define MFMA(a, b, c) __builtin_amdgcn_mfma_f32_32x32x16_bf16(__builtin_bit_cast(bf16x8, a), __builtin_bit_cast(bf16x8, b), (c), 0, 0, 0)
#define RLX_AGENT __ATOMIC_RELAXED, __HIP_MEMORY_SCOPE_AGENT
#define BLD(voff, soff) __builtin_amdgcn_raw_buffer_load_b128(rs, (voff), (soff), 16)
#define BST(v, voff) __builtin_amdgcn_raw_buffer_store_b128((v), rs, (voff), 0, 16)

#define POLL_SLEEP 4
#define G1PRIO 3
#define XPF_STEP 1
#define PFLAG_STEP 1

constexpr u32 WS_GEN = 0;
constexpr u32 WS_FST = 2048;
constexpr u32 WS_FT = 4096;
constexpr u32 WS_FO = WS_FT + 8192;
constexpr u32 WS_FS = WS_FO + 8192;
constexpr u32 WS_FP = WS_FS + 8192;
constexpr u32 WS_T = 65536;
constexpr u32 WS_O = WS_T + 6291456;
constexpr u32 WS_P = WS_O + 2 * 2097152;
constexpr u32 WS_END = WS_P + 16 * 86016;
constexpr u32 WS_STAMP = 16u << 20;

constexpr u32 SLOT_B = 30720, SLOT_HALO = 16384, SLOT_W = 18432;
constexpr u32 L_Q = 4 * SLOT_B;
constexpr u32 L_X = L_Q + 8192;
constexpr u32 L_WO = 0, WO_B = 50176;
constexpr u32 L_SCR = 2 * WO_B;
constexpr u32 L_LP = L_SCR + 57344;
constexpr u32 L_SYNC = L_LP + 4096;
constexpr u32 LDS_BYTES = 163840;

constexpr float SCALE = 0.18898223650461362f;
constexpr float LOG2E = 1.4426950408889634f;
constexpr float QSC = SCALE * LOG2E;
constexpr float MSHIFT = 40.0f;
constexpr u32 SPIN_CAP = 1u << 17;
constexpr u32 SENT = 0xFFFFFFFFu;

DI u32 pk(float lo, float hi) { f32x2 v = {lo, hi}; return __builtin_bit_cast(u32, __builtin_convertvector(v, bf2_t)); }
DI u32x4 pk8(const f32x16& a, int b) { u32x4 r = {pk(a[b], a[b + 1]), pk(a[b + 2], a[b + 3]), pk(a[b + 4], a[b + 5]), pk(a[b + 6], a[b + 7])}; return r; }
DI float blo(u32 x) { return __uint_as_float(x << 16); }
DI float bhi(u32 x) { return __uint_as_float(x & 0xffff0000u); }
DI float sel(bool c, float a, float b) { PIN(a); PIN(b); return c ? a : b; }
DI float swapmax(float m) { auto r = __builtin_amdgcn_permlane32_swap(__float_as_uint(m), __float_as_uint(m), false, false); return fmaxf(__uint_as_float(r[0]), __uint_as_float(r[1])); }
DI float swapadd(float m) { auto r = __builtin_amdgcn_permlane32_swap(__float_as_uint(m), __float_as_uint(m), false, false); return __uint_as_float(r[0]) + __uint_as_float(r[1]); }
DI void glds16(u32 voff, const void* sbase, u32 ldsaddr) {
  u32 keep;
  asm volatile("s_mov_b32 %0, m0\n\ts_mov_b32 m0, %3\n\ts_nop 0\n\tglobal_load_lds_dwordx4 %1, %2\n\ts_mov_b32 m0, %0"
               : "=&s"(keep) : "v"(voff), "s"(sbase), "s"(ldsaddr) : "memory");
}
DI void glds16_sc1(u32 voff, const void* sbase, u32 ldsaddr) {
  u32 keep;
  asm volatile("s_mov_b32 %0, m0\n\ts_mov_b32 m0, %3\n\ts_nop 0\n\tglobal_load_lds_dwordx4 %1, %2 sc1\n\ts_mov_b32 m0, %0"
               : "=&s"(keep) : "v"(voff), "s"(sbase), "s"(ldsaddr) : "memory");
}
DI void touch_l2(u32 voff, const void* sbase, u32 ldsaddr) {
  u32 keep;
  asm volatile("s_mov_b32 %0, m0\n\ts_mov_b32 m0, %3\n\ts_nop 0\n\tglobal_load_lds_dword %1, %2\n\ts_mov_b32 m0, %0"
               : "=&s"(keep) : "v"(voff), "s"(sbase), "s"(ldsaddr) : "memory");
}
DI void glds63(u32 voff, const void* sbase, u32 ldsaddr) {
  u32 keep;
  asm volatile("s_mov_b32 %0, m0\n\ts_mov_b32 m0, %3\n\ts_bitset0_b32 exec_hi, 31\n\tglobal_load_lds_dwordx4 %1, %2\n\ts_bitset1_b32 exec_hi, 31\n\ts_mov_b32 m0, %0"
               : "=&s"(keep) : "v"(voff), "s"(sbase), "s"(ldsaddr) : "memory");
}
DI u32x4 ldsr128(const LAS char* p) { return *(const LAS u32x4*)p; }
DI void ldsw128(LAS char* p, u32x4 v) { *(LAS u32x4*)p = v; }
typedef short v4i16_t __attribute__((ext_vector_type(4)));
DI u32x2 ldstr(const LAS char* p) { return __builtin_bit_cast(u32x2, __builtin_amdgcn_ds_read_tr16_b64_v4i16((LAS v4i16_t*)p)); }
DI u32x4 trfrag(const LAS char* p0, const LAS char* p1) { u32x2 a = ldstr(p0), b = ldstr(p1); u32x4 r = {a[0], a[1], b[0], b[1]}; return r; }
#define LBAR() do { __builtin_amdgcn_s_waitcnt(0xC07F); asm volatile("s_barrier" ::: "memory"); SBAR(); } while (0)

DI void lds_set(LAS char* p) { asm volatile("" ::: "memory"); __hip_atomic_store((LAS u32*)p, 1u, __ATOMIC_RELAXED, __HIP_MEMORY_SCOPE_WORKGROUP); asm volatile("" ::: "memory"); }
DI void lds_wait(const LAS char* p, u32 want) {
#pragma unroll 1
  for (u32 spins = 0; spins < (SPIN_CAP << 4); ++spins) {
    const u32 v = __hip_atomic_load((const LAS u32*)p, __ATOMIC_RELAXED, __HIP_MEMORY_SCOPE_WORKGROUP);
    if (__all(v == want)) break;
    __builtin_amdgcn_s_sleep(1);
  }
  asm volatile("" ::: "memory");
}
DI void wait_flags(const char* ws, u32 laneoff, u64 tag) {
  const GAS u64* f = (const GAS u64*)(ws + laneoff);
#pragma unroll 1
  for (u32 spins = 0; spins < SPIN_CAP; ++spins) {
    u64 v = __hip_atomic_load(f, RLX_AGENT);
    if (__all(v == tag)) break;
    __builtin_amdgcn_s_sleep(POLL_SLEEP);
  }
  __builtin_amdgcn_fence(__ATOMIC_ACQUIRE, "wavefront");
  asm volatile("" ::: "memory");
}
DI void set_flag(char* ws, u32 off, u64 tag) { __hip_atomic_store((GAS u64*)(ws + off), tag, RLX_AGENT); }

#define ST(i) do {} while (0)

#define TP(i) do {} while (0)
#define LWAIT(N, c) asm volatile("s_waitcnt vmcnt(" #N ")\n\ts_barrier" ::: "memory")
DI void lds_add1(LAS char* p, int lane) { asm volatile("" ::: "memory"); if (lane == 0) __hip_atomic_fetch_add((LAS u32*)p, 1u, __ATOMIC_RELAXED, __HIP_MEMORY_SCOPE_WORKGROUP); asm volatile("" ::: "memory"); }
#define SLOTADDR(c) do { const u32 sb_ = (u32)((c) & 3) * SLOT_B; xa = sb_ + xa0; xh = sb_ + xh0; wp = lds + sb_ + wa0; } while (0)
#define RDX(j) x[j] = ldsr128(lds + (xa ^ ((j) << 5)))
#define RDH(j) do { if (HALO) hx[j] = ldsr128(lds + (xh ^ ((j) << 5))); } while (0)
#define RDW(t, j) f[t][j] = PACKED ? ldsr128(wp + (j) * 3072 + (t) * 1024) : trfrag(wp + (j) * 3072 + (t) * 56, wp + (j) * 3072 + 768 + (t) * 56)
#define MM(A, t, j) A = ((t) >= 3 - NV) ? MFMA(x[j], f[t][j], A) : MFMA(f[t][j], x[j], A)
#define MH(j) do { if (HALO == 1) ah = MFMA(f[1][j], hx[j], ah); if (HALO == 2) ah = MFMA(hx[j], f[2][j], ah); } while (0)
#define HALF(m, r, I0, I1, I2, I3) do { \
    MM(a0, 0, m); SBAR(); RDX(r); RDW(0, r); SBAR(); \
    MM(a1, 1, m); SBAR(); RDW(1, r); I0; SBAR(); \
    MM(a2, 2, m); SBAR(); RDW(2, r); RDH(r); I1; SBAR(); \
    MH(m); SBAR(); \
    MM(a0, 0, m + 1); SBAR(); RDX(r + 1); RDW(0, r + 1); I2; SBAR(); \
    MM(a1, 1, m + 1); SBAR(); RDW(1, r + 1); RDW(2, r + 1); RDH(r + 1); SBAR(); \
    MM(a2, 2, m + 1); SBAR(); I3; SBAR(); \
    MH(m + 1); SBAR(); } while (0)
#define RDFIRST() do { RDX(0); RDW(0, 0); RDW(1, 0); RDW(2, 0); RDH(0); RDX(1); RDW(0, 1); RDW(1, 1); RDW(2, 1); RDH(1); SBAR(); } while (0)

template <int NV>
DI void gemmB(LAS char* lds, u32 xa0, u32 wa0, f32x16& a0, f32x16& a1, f32x16& a2, const char* ws, int fstep, u32 flagoff, u64 tag, int lane) {
  constexpr bool PACKED = false; constexpr int HALO = 0;
  u32x4 x[4], f[3][4], hx[4]; f32x16 ah;
  u32 xa, xh; const u32 xh0 = 0; const LAS char* wp;
  LBAR();
  SLOTADDR(0);
  RDFIRST();
#pragma unroll 1
  for (int c = 0; c < 7; ++c) {
    HALF(0, 2, , , , );
    asm volatile("s_cmp_eq_u32 %0, %1\n\ts_cbranch_scc0 1f\n\ts_waitcnt vmcnt(0)\n\ts_mov_b64 exec, 3\n\tglobal_store_dwordx2 %2, %3, %4 sc1\n\ts_mov_b64 exec, -1\n1:"
                 :: "s"(c), "s"(fstep), "v"(flagoff), "v"(tag), "s"(ws) : "memory", "scc");
    __builtin_amdgcn_s_waitcnt(0xC07F);
    asm volatile("s_cmp_eq_u32 %0, 6\n\ts_cbranch_scc1 1f\n\ts_barrier\n1:" :: "s"(c) : "memory", "scc");
    SBAR();
    SLOTADDR(c + 1);
    HALF(2, 0, , , , );
  }
}

template <int HALO>
DI void gemmA(LAS char* lds, u32 xa0, u32 xh0, u32 wa0, f32x16& a0, f32x16& a1, f32x16& a2, f32x16& ah) {
  constexpr bool PACKED = true; constexpr int NV = 1;
  u32x4 x[4], f[3][4], hx[4];
  u32 xa, xh; const LAS char* wp;
  SLOTADDR(7);
  RDFIRST();
#pragma unroll 1
  for (int c = 7; c < 14; ++c) {
    HALF(0, 2, , , , );
    __builtin_amdgcn_s_waitcnt(0xC07F);
    asm volatile("s_waitcnt vmcnt(0)\n\ts_cmp_eq_u32 %0, 13\n\ts_cbranch_scc1 1f\n\ts_barrier\n1:" :: "s"(c) : "memory", "scc");
    SBAR();
    SLOTADDR(c + 1);
    HALF(2, 0, , , , );
  }
}

__global__ __launch_bounds__(512, 2) void fused_attn(const char* __restrict__ Ax, const char* __restrict__ Aw, const char* __restrict__ Ao,
                                                     const char* __restrict__ Bx, const char* __restrict__ Bw, const char* __restrict__ Bo,
                                                     char* __restrict__ out, char* ws) {
  __shared__ __attribute__((aligned(16))) char lds_raw[LDS_BYTES];
  LAS char* lds = (LAS char*)lds_raw;
  const u32 lds0 = (u32)(size_t)lds;
  const int tid = threadIdx.x, lane = tid & 63, r32 = lane & 31, hh = lane >> 5;
  const int w = __builtin_amdgcn_readfirstlane(tid >> 6);
  const int bid = blockIdx.x, xcd = bid & 7, slot = bid >> 3;
  const int rb = 4 * (xcd & 3) + (slot & 3), g = 8 * (xcd >> 2) + (slot >> 2);
  const int h = 2 * xcd + (slot >> 4), qb = slot & 15;
  const int cg = slot & 7, ot0 = 8 * xcd + 2 * (slot >> 3);
  const int tq = w & 3;
  asm volatile("" :: "s"(Ax), "s"(Aw), "s"(Ao), "s"(Bx), "s"(Bw), "s"(Bo), "s"(out), "s"(ws));
  ST(0);
  u64 stv = 0;
  const bool packer = w < 3 && rb < 14;
  if (packer) {
    const u32 vo = (u32)(32 * rb + (lane >> 2)) * 2688 + tq * 896 + 56 * g - 8 * (g & 1) + (lane & 3) * 16;
    glds16(vo, Aw, lds0 + L_X + tq * 2560); glds16(vo, Aw + 16 * 2688, lds0 + L_X + tq * 2560 + 1024);
  }
  int kc = (g & 7) + 2 * (rb & 3); kc = kc >= 7 ? kc - 7 : kc;
  const int p0 = (tq & 1) + 8 * (tq >> 1);
  const u32 voffX = (u32)(128 * rb + 8 * p0 + (lane >> 3)) * 896u + (u32)(((lane & 7) ^ ((4 * (tq & 1) + (lane >> 4)) & 7)) << 4);
  { const int j0 = (w >> 2) * 2;
    glds16(voffX, Bx + kc * 128 + j0 * 14336, lds0 + (p0 + 2 * j0) * 1024);
    glds16(voffX, Bx + kc * 128 + (j0 + 1) * 14336, lds0 + (p0 + 2 * j0 + 2) * 1024); }
  u32 voffW[3];
#pragma unroll
  for (int j = 0; j < 3; ++j) { const int i = 64 * j + lane, kr = (i * 171) >> 11, rem = i - kr * 12; voffW[j] = (16 * tq + kr) * 2688 + 168 * g - 8 * (g & 1) + min(rem, 10) * 16; }
  if (w >= 4) { glds16(voffW[0], Bw + kc * (64 * 2688), lds0 + SLOT_W + (3 * tq) * 1024); glds16(voffW[1], Bw + kc * (64 * 2688), lds0 + SLOT_W + (3 * tq + 1) * 1024); }
  else glds16(voffW[2], Bw + kc * (64 * 2688), lds0 + SLOT_W + (3 * tq + 2) * 1024);
  kc = kc == 6 ? 0 : kc + 1;

  const __amdgpu_buffer_rsrc_t rs = __builtin_amdgcn_make_buffer_rsrc(ws, 0, WS_END, 0x00020000);
  const f32x16 zero16 = {};
  u64 tag;
  u32x4 qf[4][2];
  u32x4 kv[8][4];
  const u32 kvb = WS_T + (u32)(((16 + h) * 64 + 8 * w) * 2) * 1024 + lane * 16;
  const int trot = qb >> 1;
  const int ptile = 4 * rb + tq;
#define LOADF(t, f) kv[t][f] = BLD(kvb + ((f) & 1) * 1024, (((t) + trot) & 7) * 2048 + ((f) >> 1) * 2097152)
#define LOADT(t) do { LOADF(t, 0); LOADF(t, 1); LOADF(t, 2); LOADF(t, 3); } while (0)
#define READQ() do { _Pragma("unroll") for (int i_ = 0; i_ < 4; ++i_) { const u32 a_ = L_Q + (u32)((i_ ^ (w >> 1)) * 2048) + lane * 16; \
      qf[i_][0] = ldsr128(lds + a_); qf[i_][1] = ldsr128(lds + a_ + 1024); } } while (0)
#define GETTAG() do { const u64 gen_ = *(const __attribute__((address_space(4))) u64*)(ws + WS_GEN); \
      tag = gen_ * 6364136223846793005ull + 1442695040888963407ull; asm volatile("" : "+s"(tag)); } while (0)
  u32 voffH;
  { const int hr = 8 * (tq & 1) + (lane >> 3); int tok = 128 * rb - 16 + hr; tok = tok < 0 ? 0 : tok;
    voffH = (u32)tok * 896u + (u32)(((lane & 7) ^ ((hr >> 1) & 7)) << 4); }
  auto issueA = [&](int cc) {
    const u32 sb = lds0 + (u32)(cc & 3) * SLOT_B;
    const char* xs = Ax + kc * 128; const char* wq = ws + WS_P + g * 86016 + kc * 12288 + tq * 3072;
    kc = kc == 6 ? 0 : kc + 1;
#pragma unroll
    for (int j = 0; j < 4; ++j) glds16(voffX, xs + j * 14336, sb + (p0 + 2 * j) * 1024);
    if (tq < 2) glds16(voffH, xs, sb + SLOT_HALO + tq * 1024);
#pragma unroll
    for (int j = 0; j < 3; ++j) glds16_sc1((u32)lane * 16, wq + j * 1024, sb + SLOT_W + (3 * tq + j) * 1024);
  };
  const int xrow = 32 * tq + r32;
  const u32 xa0 = xrow * 128 + ((hh ^ ((xrow >> 1) & 7)) << 4);
  const u32 xh0 = SLOT_HALO + (r32 & 15) * 128 + ((hh ^ (((r32 & 15) >> 1) & 7)) << 4);
  f32x16 aq = zero16, ak = zero16, av = zero16, ah = zero16;

  if (w >= 4) {
    auto issueB = [&](int cc) {
      const u32 sb = lds0 + (u32)(cc & 3) * SLOT_B;
      const char* xs = Bx + kc * 128; const char* wq = Bw + kc * (64 * 2688);
      kc = kc == 6 ? 0 : kc + 1;
#pragma unroll
      for (int j = 0; j < 4; ++j) {
        glds16(voffX, xs + j * 14336, sb + (p0 + 2 * j) * 1024);
        if (j < 3) glds16(voffW[j], wq, sb + SLOT_W + (3 * tq + j) * 1024);
      }
    };
    issueB(1);
    GETTAG();
    const u32 pflag = WS_FP + (u32)(g * 64 + (lane < 42 ? lane : 0)) * 8;
    u64 pfv = 0;
#pragma unroll 1
    for (int c = 0; c < 5; ++c) {
      LWAIT(7, c);
      asm volatile("s_cmp_eq_u32 %3, %4\n\ts_cbranch_scc0 1f\n\tglobal_load_dwordx2 %0, %1, %2 sc1\n1:" : "+v"(pfv) : "v"(pflag), "s"(ws), "s"(c), "s"(w) : "memory", "scc");
      issueB(c + 2);
      if (c == XPF_STEP) {
        int row0 = 128 * rb - 16 + 18 * (g & 7); row0 = row0 < 0 ? 0 : row0;
        touch_l2((u32)row0 * 896u + (u32)min(tq * 32 + r32, 125) * 128u, Ax, lds0 + L_SYNC + 1024);
      }
    }
    asm volatile("s_waitcnt vmcnt(7)" : "+v"(pfv) :: "memory");
    if (w == 4 && !__all(pfv == tag)) wait_flags(ws, pflag, tag);
    LWAIT(7, 5);
    issueA(7);
    LWAIT(7, 6);
    issueA(8);
    ST(1);
    asm volatile("s_waitcnt vmcnt(7)\n\ts_barrier" ::: "memory");
    { const u32 wp0 = SLOT_W + lane * 16;
      if (tq == 2) gemmA<1>(lds, xa0, xh0, wp0, aq, ak, av, ah);
      else if (tq == 3) gemmA<2>(lds, xa0, xh0, wp0, aq, ak, av, ah);
      else gemmA<0>(lds, xa0, xh0, wp0, aq, ak, av, ah); }
    ST(2);
    TP(60);
    __builtin_amdgcn_s_setprio(3);
    LAS char* xb = lds + L_X;
    u32x4 aq0 = pk8(aq, 0), aq1 = pk8(aq, 8), ak0 = pk8(ak, 0), ak1 = pk8(ak, 8), cv0 = pk8(av, 0), cv1 = pk8(av, 8);
    if (hh) { aq1[2] = 0; aq1[3] = 0; ak1[2] = 0; ak1[3] = 0; }
    if (r32 >= 28) { cv0 = (u32x4){0, 0, 0, 0}; cv1 = cv0; }
    if (tq < 3) { LAS char* p = xb + (tq + 1) * 3072 + lane * 16; ldsw128(p, ak0); ldsw128(p + 1024, ak1); ldsw128(p + 2048, cv1); }
    if (tq == 2) {
      u32x4 h0 = pk8(ah, 0), h1 = pk8(ah, 8);
      if (hh) { h1[2] = 0; h1[3] = 0; }
      ldsw128(xb + lane * 16, h0); ldsw128(xb + 1024 + lane * 16, h1);
    } else if (tq == 3) {
      u32x4 h1 = pk8(ah, 8);
      if (r32 >= 28) h1 = (u32x4){0, 0, 0, 0};
      ldsw128(xb + 2048 + lane * 16, h1);
    }
    lds_set(lds + L_SYNC + 16 + tq * 4);
    f32x16 sc = MFMA(ak0, aq0, zero16); sc = MFMA(ak1, aq1, sc);
    lds_wait(lds + L_SYNC + 16 + (lane & 3) * 4, 1);
    u32x4 pk0 = ldsr128(xb + tq * 3072 + lane * 16), pk1 = ldsr128(xb + tq * 3072 + 1024 + lane * 16), pv1 = ldsr128(xb + tq * 3072 + 2048 + lane * 16);
    f32x16 sp = MFMA(pk0, aq0, zero16); sp = MFMA(pk1, aq1, sp);
    const bool odd = r32 & 1; const bool pvalid = ptile != 0;
    float t[12];
    float mx = -INFINITY;
#pragma unroll
    for (int i = 0; i < 12; ++i) {
      const int rp = i < 8 ? 2 * i : 2 * i - 8;
      const float sv = i < 8 ? sel(odd, sc[rp + 1], sc[rp]) : sel(odd, sp[rp + 1], sp[rp]);
      const int rho = (rp & 3) + 8 * (rp >> 2) + 4 * hh;
      const int dl = (r32 & ~1) - rho + (i < 8 ? 0 : 32);
      const bool ok = (unsigned)dl <= 14u && (i < 8 || pvalid);
      const float l1 = blo(pk(sv, 0.f)) * SCALE;
      const float l2 = blo(pk(l1, 0.f));
      t[i] = ok ? l2 : -INFINITY;
      mx = fmaxf(mx, t[i]);
    }
    mx = swapmax(mx);
    float sum = 0.f;
#pragma unroll
    for (int i = 0; i < 12; ++i) { t[i] = __builtin_amdgcn_exp2f((t[i] - mx) * LOG2E); sum += t[i]; }
    sum = swapadd(sum);
    const float inv = __builtin_amdgcn_rcpf(sum);
    u32 pw[12];
#pragma unroll
    for (int i = 0; i < 12; ++i) { const u32 b = pk(t[i] * inv, 0.f); pw[i] = odd ? (b << 16) : b; }
    const u32x4 p0 = {pw[0], pw[1], pw[2], pw[3]}, p1 = {pw[4], pw[5], pw[6], pw[7]}, pp = {pw[8], pw[9], pw[10], pw[11]};
    f32x16 oa = MFMA(cv0, p0, zero16); oa = MFMA(cv1, p1, oa); oa = MFMA(pv1, pp, oa);
    BST(pk8(oa, 0), WS_O + (u32)(ptile * 16 + g) * 2048 + lane * 16);
    BST(pk8(oa, 8), WS_O + (u32)(ptile * 16 + g) * 2048 + 1024 + lane * 16);
    lds_wait(lds + L_SYNC + (lane & 3) * 4, 1);
    LOADT(0); LOADT(1);
    READQ();
  } else {
    if (tid < 16) *(LAS u32*)(lds + L_SYNC + tid * 4) = 0;
    GETTAG();
    asm volatile("s_waitcnt vmcnt(3)" ::: "memory");
    if (packer) {
      const LAS char* pp = lds + L_X + tq * 2560 + (8 * hh + ((lane & 15) >> 2)) * 64 + 8 * (g & 1) + ((lane >> 4) & 1) * 32 + (lane & 3) * 8;
      const u32x4 f0 = trfrag(pp, pp + 256), f1 = trfrag(pp + 1024, pp + 1280);
      const u32 po = WS_P + (u32)(g * 84 + 6 * rb + tq) * 1024 + lane * 16;
      BST(f0, po); BST(f1, po + 3072);
      asm volatile("s_waitcnt vmcnt(2)" ::: "memory");
    } else asm volatile("s_waitcnt vmcnt(0)" ::: "memory");
    { const u32 wa0 = SLOT_W + (8 * hh + ((lane & 15) >> 2)) * 192 + 8 * (g & 1) + ((lane >> 4) & 1) * 32 + (lane & 3) * 8;
      const int fs = (packer || w == 0) ? PFLAG_STEP : 99;
      const u32 fo = (packer && !(w == 0 && lane == 1)) ? WS_FP + (u32)(g * 64 + 3 * rb + tq) * 8 : WS_FST + (u32)bid * 8;
      if (g < 10) gemmB<0>(lds, xa0, wa0, aq, ak, av, ws, fs, fo, tag, lane);
      else if (g == 10) gemmB<1>(lds, xa0, wa0, aq, ak, av, ws, fs, fo, tag, lane);
      else gemmB<3>(lds, xa0, wa0, aq, ak, av, ws, fs, fo, tag, lane); }
    ST(1);
    __builtin_amdgcn_s_setprio(3);
    const u32 to = WS_T + (u32)((3 * g * 64 + ptile) * 2) * 1024 + lane * 16;
    auto put = [&](f32x16& a, int j) {
      const int sl = 3 * g + j;
      if (sl < 16) a *= QSC;
      u32x4 t0 = pk8(a, 0), t1 = pk8(a, 8);
      if (sl < 32) { if (hh) { t1[2] = sl < 16 ? 0u : 0x00003F80u; t1[3] = 0; } }
      else if (r32 >= 28) { const u32 f = r32 == 28 ? 0x3F803F80u : 0u; t0 = (u32x4){f, f, f, f}; t1 = t0; }
      BST(t0, to + j * 131072); BST(t1, to + j * 131072 + 1024);
    };
    kc = kc == 6 ? 0 : kc + 1;
    __builtin_amdgcn_s_waitcnt(0xC07F);
    LWAIT(63, 7);
    put(aq, 0); put(ak, 1); put(av, 2);
    LWAIT(63, 8);
    issueA(9);
    asm volatile("s_waitcnt vmcnt(7)" ::: "memory");
    u32 prevs;
    { u32 prev = 0;
      if (lane == 0) prev = __hip_atomic_fetch_add((LAS u32*)(lds + L_SYNC + 40), 1u, __ATOMIC_RELAXED, __HIP_MEMORY_SCOPE_WORKGROUP);
      prevs = __builtin_amdgcn_readfirstlane(prev);
      if (prevs == 3 && lane < 3) set_flag(ws, WS_FT + (u32)((3 * g + lane) * 16 + rb) * 8, tag); }
    issueA(10);
    const u32 tflag = WS_FT + (u32)((lane & 32) ? h * 16 + qb : (16 + (lane & 16) + h) * 16 + (lane & 15)) * 8;
    u64 tfv = 0;
    asm volatile("s_cmp_eq_u32 %0, 3\n\ts_cbranch_scc1 1f\n\ts_waitcnt vmcnt(7)\n1:" :: "s"(prevs) : "memory", "scc");
    LWAIT(8, 9);  issueA(11);
    LWAIT(7, 10); issueA(12);
    LWAIT(7, 11); issueA(13);
    LWAIT(7, 12);
    asm volatile("s_cmp_eq_u32 %3, 0\n\ts_cbranch_scc0 1f\n\tglobal_load_dwordx2 %0, %1, %2 sc1\n\ts_waitcnt vmcnt(1)\n\ts_branch 2f\n1:\n\ts_waitcnt vmcnt(0)\n2:"
                 : "+v"(tfv) : "v"(tflag), "s"(ws), "s"(w) : "memory", "scc");
    LWAIT(1, 13);
    TP(28);
    if (w == 0) {
      asm volatile("s_waitcnt vmcnt(0)" : "+v"(tfv) :: "memory");
      if (!__all(tfv == tag)) wait_flags(ws, tflag, tag);
      lds_set(lds + L_SYNC + 44);
    } else lds_wait(lds + L_SYNC + 44, 1);
    TP(29);
    u32x4 q0 = BLD(WS_T + (u32)((h * 64 + 4 * qb + tq) * 2) * 1024 + lane * 16, 0), q1 = BLD(WS_T + (u32)((h * 64 + 4 * qb + tq) * 2) * 1024 + lane * 16, 1024);
    LOADT(0); LOADT(1);
    SBAR();
    f32x16 s = MFMA(kv[0][0], q0, zero16); s = MFMA(kv[0][1], q1, s);
    ldsw128(lds + L_Q + tq * 2048 + lane * 16, q0);
    float m = s[0];
#pragma unroll
    for (int i = 1; i < 16; ++i) m = fmaxf(m, s[i]);
    m = swapmax(m);
    if (hh) q1[2] = pk(-(m + MSHIFT), 0.f);
    ldsw128(lds + L_Q + tq * 2048 + 1024 + lane * 16, q1);
    lds_set(lds + L_SYNC + tq * 4);
    ST(2);
    TP(30);
    lds_wait(lds + L_SYNC + (lane & 3) * 4, 1);
    READQ();
    TP(31);
  }
  __builtin_amdgcn_s_waitcnt(0xC07F);
  if (lane == 0) __hip_atomic_fetch_add((LAS u32*)(lds + L_SYNC + 32), 1u, __ATOMIC_RELAXED, __HIP_MEMORY_SCOPE_WORKGROUP);
  ST(3);
  const int qt2 = w & 1, kq = w >> 1, otile = ot0 + qt2;
  const u32 ofo = (u32)(otile * 16 + 4 * kq) * 2048 + lane * 16;
  u32x4 of[8];
  f32x16 o[4];
  {
    const u32 voffO = (u32)((lane / 7) * 896 + (lane % 7) * 16 + cg * 112);
    const int wr6 = w < 2 ? (w == 0 ? 432 : 439) : 9 * w + 360;
#define WOP(j) do { const int jj_ = (j) % 7, r_ = jj_ < 6 ? 9 * w + 72 * jj_ : wr6; \
      glds63(voffO, ((j) >= 7 ? Bo : Ao) + r_ * 896, lds0 + ((j) >= 7 ? WO_B : 0) + r_ * 112); } while (0)
    const u32 fao = WS_FO + (u32)(lane & 4) * 2048 + (u32)(otile * 16 + 4 * kq + (lane & 3)) * 8;
    const GAS u64* fa = (const GAS u64*)(ws + fao);
    u64 fav;
    f32x16 s[2]; u32x4 pa, pb;
    __builtin_amdgcn_s_setprio(2);
    SBAR();
    s[0] = MFMA(kv[0][0], qf[0][0], zero16); s[0] = MFMA(kv[0][1], qf[0][1], s[0]);
    SBAR();
#define EXP(x) x = __builtin_amdgcn_exp2f(x)
#define STEP(u) do { \
      constexpr int kt = (u) >> 2, qt = (u) & 3, un = (u) + 1, ktn = (un >> 2) & 7, qtn = un & 3, up = (u) > 0 ? (u) - 1 : 0, kl = (kt + 3) & 7; \
      f32x16& sc = s[(u) & 1]; f32x16& sn = s[un & 1]; \
      if (un < 32) { sn = MFMA(kv[ktn][0], qf[qtn][0], zero16); PIN(sn); } SBAR(); \
      if ((u) < 4) LOADF(2, qt); \
      if (kt + 3 < 8) LOADF(kl, qt); \
      if ((u) >= 24) of[(u) & 7] = BLD(WS_O + ofo, ((u) & 7) * 1024); \
      EXP(sc[0]); EXP(sc[1]); EXP(sc[2]); EXP(sc[3]); PIN(sc); SBAR(); \
      if ((u) > 0) { o[up & 3] = MFMA(kv[up >> 2][3], pb, o[up & 3]); PIN(o[up & 3]); } SBAR(); \
      EXP(sc[4]); EXP(sc[5]); PIN(sc); SBAR(); pa[0] = pk(sc[0], sc[1]); pa[1] = pk(sc[2], sc[3]); PIN(pa); SBAR(); EXP(sc[6]); EXP(sc[7]); PIN(sc); SBAR(); \
      if (un < 32) { sn = MFMA(kv[ktn][1], qf[qtn][1], sn); PIN(sn); } SBAR(); \
      if ((u) >= 12 && (u) < 26) WOP((u) - 12); \
      EXP(sc[8]); PIN(sc); SBAR(); pa[2] = pk(sc[4], sc[5]); pa[3] = pk(sc[6], sc[7]); PIN(pa); SBAR(); EXP(sc[9]); EXP(sc[10]); EXP(sc[11]); PIN(sc); SBAR(); \
      o[qt] = MFMA(kv[kt][2], pa, (u) < 4 ? zero16 : o[qt]); PIN(o[qt]); SBAR(); \
      EXP(sc[12]); EXP(sc[13]); EXP(sc[14]); EXP(sc[15]); PIN(sc); SBAR(); \
      pb[0] = pk(sc[8], sc[9]); pb[1] = pk(sc[10], sc[11]); pb[2] = pk(sc[12], sc[13]); pb[3] = pk(sc[14], sc[15]); PIN(pb); SBAR(); \
      if ((u) == 15) __builtin_amdgcn_s_setprio(1); \
      if ((u) == 26) __builtin_amdgcn_s_setprio(0); \
    } while (0)
#define STEP4(t) STEP(4 * (t)); STEP(4 * (t) + 1); STEP(4 * (t) + 2); STEP(4 * (t) + 3)
    STEP4(0);
    {
      const u32x4 sv = {SENT, SENT, SENT, SENT};
      asm volatile("s_cmp_lt_u32 %0, 4\n\ts_cbranch_scc0 1f\n\tbuffer_store_dwordx4 %1, %2, %3, 0 offen sc1\n\tbuffer_store_dwordx4 %1, %2, %3, 0 offen offset:1024 sc1\n\ts_nop 1\n1:"
                   :: "s"(w), "v"(sv), "v"(WS_O + 2097152 + (u32)((4 * qb + tq) * 16 + h) * 2048 + lane * 16), "s"(rs) : "memory", "scc"); }
    STEP4(1); STEP4(2);
    lds_wait(lds + L_SYNC + 16 + (lane & 3) * 4, 1);
    asm volatile("s_waitcnt vmcnt(8)\n\ts_mov_b64 exec, 1\n\tglobal_store_dwordx2 %0, %1, %2 sc1\n\ts_mov_b64 exec, -1"
                 :: "v"(w < 4 ? WS_FS + (u32)((4 * qb + tq) * 16 + h) * 8 : WS_FO + (u32)(ptile * 16 + g) * 8), "v"(tag), "s"(ws) : "memory");
    if (bid == 0 && w == 0) {
#pragma unroll 1
      for (u32 spins = 0; spins < SPIN_CAP; ++spins) {
        const u32x4 fa0 = BLD(WS_FST + lane * 32, 0), fa1 = BLD(WS_FST + lane * 32, 16);
        const u32 tl = (u32)tag, th = (u32)(tag >> 32);
        if (__all(fa0[0] == tl && fa0[1] == th && fa0[2] == tl && fa0[3] == th && fa1[0] == tl && fa1[1] == th && fa1[2] == tl && fa1[3] == th)) break;
        __builtin_amdgcn_s_sleep(4);
      }
      if (lane == 0) *(GAS u64*)(ws + WS_GEN) = tag;
    }
    STEP4(3); STEP4(4);
    fav = __hip_atomic_load(fa, RLX_AGENT);
    SBAR();
    STEP4(5);
    if (__builtin_expect(!__all(fav == tag), 0)) wait_flags(ws, fao, tag);
    asm volatile("" ::: "memory");
    STEP4(6);
    lds_wait(lds + L_SYNC + 32, 8);
    STEP4(7);
    o[3] = MFMA(kv[7][3], pb, o[3]);
  }
  ST(4);
  {
    const int ws1 = w & 1;
    { const u32x4 a = pk8(o[0], 0), b = pk8(o[0], 8); const u32x4 oth = ws1 ? a : b;
      ldsw128(lds + L_SCR + (u32)((w ^ 1) * 7 + 0) * 1024 + lane * 16, oth); }
#pragma unroll
    for (int i = 1; i < 4; ++i)
#pragma unroll
      for (int s2 = 0; s2 < 2; ++s2) {
        const int owner = ((i ^ (w >> 1)) << 1) | s2;
        ldsw128(lds + L_SCR + (u32)(owner * 7 + (w ^ owner) - 1) * 1024 + lane * 16, pk8(o[i], 8 * s2));
      }
    if (hh) {
#pragma unroll
      for (int i = 0; i < 4; ++i) *(LAS float*)(lds + L_LP + (u32)(((i ^ (w >> 1)) * 32 + r32) * 32 + w * 4)) = o[i][12];
    }
    float own[8];
#pragma unroll
    for (int j = 0; j < 8; ++j) own[j] = sel(ws1, o[0][8 + j], o[0][j]);
    asm volatile("s_waitcnt vmcnt(6)" ::: "memory");
    LBAR();
    ST(5);
    const f32x4 la = *(const LAS f32x4*)(lds + L_LP + (u32)(((w >> 1) * 32 + r32) * 32)), lb = *(const LAS f32x4*)(lds + L_LP + (u32)(((w >> 1) * 32 + r32) * 32 + 16));
    float l = ((la[0] + la[1]) + (la[2] + la[3])) + ((lb[0] + lb[1]) + (lb[2] + lb[3]));
#pragma unroll
    for (int k = 0; k < 7; ++k) {
      const u32x4 v = ldsr128(lds + L_SCR + (u32)(w * 7 + k) * 1024 + lane * 16);
#pragma unroll
      for (int j = 0; j < 4; ++j) { own[2 * j] += blo(v[j]); own[2 * j + 1] += bhi(v[j]); }
    }
    float chk = 0.f;
#pragma unroll
    for (int j = 0; j < 8; ++j) chk = __builtin_fmaf(own[j], 0.f, chk);
    const bool good = (l >= 0x1p-70f) && (l <= 0x1p126f) && (chk == 0.f);
    if (__builtin_expect(!__all(good), 0)) {
      u32x4 q0 = qf[0][0], q1 = qf[0][1];
      if (hh) q1[2] = 0;
      const u32 kb = WS_T + (u32)((16 + h) * 64 * 2) * 1024 + lane * 16;
      float m = -INFINITY;
#pragma unroll 1
      for (int t = 0; t < 64; ++t) {
        const u32x4 k0 = BLD(kb, t * 2048), k1 = BLD(kb + 1024, t * 2048);
        f32x16 s = MFMA(k0, q0, zero16); s = MFMA(k1, q1, s);
#pragma unroll
        for (int i = 0; i < 16; ++i) m = fmaxf(m, s[i]);
      }
      m = swapmax(m);
      f32x16 oo = zero16;
#pragma unroll 1
      for (int t = 0; t < 64; ++t) {
        const u32x4 k0 = BLD(kb, t * 2048), k1 = BLD(kb + 1024, t * 2048), v0 = BLD(kb + 2097152, t * 2048), v1 = BLD(kb + 2097152 + 1024, t * 2048);
        f32x16 s = MFMA(k0, q0, zero16); s = MFMA(k1, q1, s);
#pragma unroll
        for (int i = 0; i < 16; ++i) s[i] = __builtin_amdgcn_exp2f(s[i] - m);
        oo = MFMA(v0, pk8(s, 0), oo); oo = MFMA(v1, pk8(s, 8), oo);
      }
      { auto r = __builtin_amdgcn_permlane32_swap(__float_as_uint(oo[12]), __float_as_uint(oo[12]), false, false); l = __uint_as_float(r[1]); }
#pragma unroll
      for (int j = 0; j < 8; ++j) own[j] = sel(ws1, oo[8 + j], oo[j]);
    }
    const float inv = __builtin_amdgcn_rcpf(l);
    u32x4 ob = {pk(own[0] * inv, own[1] * inv), pk(own[2] * inv, own[3] * inv), pk(own[4] * inv, own[5] * inv), pk(own[6] * inv, own[7] * inv)};
    if (ws1 && hh) { ob[2] = 0; ob[3] = 0; }
#pragma unroll
    for (int j = 0; j < 4; ++j) ob[j] = min(ob[j], SENT - 1);
    BST(ob, WS_O + 2097152 + (u32)(((4 * qb + (w >> 1)) * 16 + h) * 2 + ws1) * 1024 + lane * 16);
  }
  ST(6);
  const u32 wob = (u32)(kq * 4 * 3136 + ((lane & 15) >> 2) * 112 + ((lane >> 4) & 1) * 32 + (lane & 3) * 8);
  LAS char* pw_ = lds + L_SCR + (u32)((kq * 64 + qt2 * 32 + r32) * 240 + hh * 16);
  const int trow = tid / 7, tg = tid - trow * 7;
  const LAS char* pr_ = lds + L_SCR + (u32)(trow * 240 + tg * 32);
  char* op = out + (size_t)(32 * ot0 + trow) * 1792 + cg * 112 + tg * 16;
#pragma unroll 1
  for (int br = 0; br < 2; ++br) {
    const LAS char* wb = lds + L_WO + br * WO_B + wob;
    const LAS char* wa = wb + hh * 448;
    u32x4 wf[16];
#pragma unroll
    for (int i = 0; i < 4; ++i) {
      wf[4 * i + 0] = trfrag(wa + i * 3136, wa + i * 3136 + 896);
      wf[4 * i + 1] = trfrag(wa + 56 + i * 3136, wa + 56 + i * 3136 + 896);
      wf[4 * i + 2] = trfrag(wa + i * 3136 + 1792, wb + i * 3136 + 2688);
      wf[4 * i + 3] = trfrag(wa + 56 + i * 3136 + 1792, wb + 56 + i * 3136 + 2688);
    }
    if (br == 1) {
#pragma unroll 1
      for (u32 spins = 0; spins < SPIN_CAP; ++spins) {
#pragma unroll
        for (int i = 0; i < 8; ++i) of[i] = BLD(WS_O + 2097152 + ofo, i * 1024);
        u32 mxw = 0;
#pragma unroll
        for (int i = 0; i < 8; ++i) mxw = max(max(mxw, max(of[i][0], of[i][1])), max(of[i][2], of[i][3]));
        if (__all(mxw != SENT)) break;
        __builtin_amdgcn_s_sleep(2);
      }
      ST(8);
    }
    f32x16 c0 = zero16, c1 = zero16;
#pragma unroll
    for (int i = 0; i < 4; ++i) {
      c0 = MFMA(wf[4 * i + 0], of[2 * i], c0); c1 = MFMA(wf[4 * i + 1], of[2 * i], c1);
      c0 = MFMA(wf[4 * i + 2], of[2 * i + 1], c0); c1 = MFMA(wf[4 * i + 3], of[2 * i + 1], c1);
    }
    if (br == 0) LBAR();
#pragma unroll
    for (int g = 0; g < 4; ++g) {
      if (g < 3 || !hh) {
        *(LAS f32x4*)(pw_ + g * 32) = (f32x4){c0[4 * g], c0[4 * g + 1], c0[4 * g + 2], c0[4 * g + 3]};
        *(LAS f32x4*)(pw_ + 112 + g * 32) = (f32x4){c1[4 * g], c1[4 * g + 1], c1[4 * g + 2], c1[4 * g + 3]};
      }
    }
    LBAR();
    if (w < 7) {
      f32x4 ra = *(const LAS f32x4*)pr_, rc = *(const LAS f32x4*)(pr_ + 16);
#pragma unroll
      for (int k = 1; k < 4; ++k) { ra += *(const LAS f32x4*)(pr_ + k * 15360); rc += *(const LAS f32x4*)(pr_ + k * 15360 + 16); }
      *(u32x4*)(op + br * 896) = (u32x4){pk(ra[0], ra[1]), pk(ra[2], ra[3]), pk(rc[0], rc[1]), pk(rc[2], rc[3])};
    }
    if (br == 0) LBAR();
    ST(7 + 2 * br);
  }
}

extern "C" void kernel_launch(void* const* d_in, const int* in_sizes, int n_in, void* d_out, int out_size, void* d_ws, size_t ws_size,
                              hipStream_t stream) {
  fused_attn<<<256, 512, 0, stream>>>((const char*)d_in[0], (const char*)d_in[1], (const char*)d_in[2], (const char*)d_in[3],
                                      (const char*)d_in[4], (const char*)d_in[5], (char*)d_out, (char*)d_ws);
}
```

```cpp
#define ROT 4
#include <hip/hip_runtime.h>
#include <hip/hip_bf16.h>

typedef unsigned u32;
typedef unsigned long long u64;
typedef short bf16x8 __attribute__((ext_vector_type(8)));
typedef float f32x16 __attribute__((ext_vector_type(16)));
typedef float f32x4 __attribute__((ext_vector_type(4)));
typedef float f32x2 __attribute__((ext_vector_type(2)));
typedef u32 u32x4 __attribute__((ext_vector_type(4)));
typedef u32 u32x2 __attribute__((ext_vector_type(2)));
typedef __bf16 bf2_t __attribute__((ext_vector_type(2)));
#define LAS __attribute__((address_space(3)))
#define GAS __attribute__((address_space(1)))
#define DI __device__ __forceinline__
#define SBAR() __builtin_amdgcn_sched_barrier(0)
#define PIN(x) asm volatile("" : "+v"(x))
#define MFMA(a, b, c) __builtin_amdgcn_mfma_f32_32x32x16_bf16(__builtin_bit_cast(bf16x8, a), __builtin_bit_cast(bf16x8, b), (c), 0, 0, 0)
#define RLX_AGENT __ATOMIC_RELAXED, __HIP_MEMORY_SCOPE_AGENT
#define BLD(voff, soff) __builtin_amdgcn_raw_buffer_load_b128(rs, (voff), (soff), 16)
#define BST(v, voff) __builtin_amdgcn_raw_buffer_store_b128((v), rs, (voff), 0, 16)

#define POLL_SLEEP 4
#define G1PRIO 3
#define XPF_STEP 1
#define PFLAG_STEP 1

constexpr u32 WS_GEN = 0;
constexpr u32 WS_FST = 2048;
constexpr u32 WS_FT = 4096;
constexpr u32 WS_FO = WS_FT + 8192;
constexpr u32 WS_FS = WS_FO + 8192;
constexpr u32 WS_FP = WS_FS + 8192;
constexpr u32 WS_T = 65536;
constexpr u32 WS_O = WS_T + 6291456;
constexpr u32 WS_P = WS_O + 2 * 2097152;
constexpr u32 WS_END = WS_P + 16 * 86016;
constexpr u32 WS_STAMP = 16u << 20;

constexpr u32 SLOT_B = 30720, SLOT_HALO = 16384, SLOT_W = 18432;
constexpr u32 L_Q = 4 * SLOT_B;
constexpr u32 L_X = L_Q + 8192;
constexpr u32 L_WO = 0, WO_B = 50176;
constexpr u32 L_SCR = 2 * WO_B;
constexpr u32 L_LP = L_SCR + 57344;
constexpr u32 L_SYNC = L_LP + 4096;
constexpr u32 LDS_BYTES = 163840;

constexpr float SCALE = 0.18898223650461362f;
constexpr float LOG2E = 1.4426950408889634f;
constexpr float QSC = SCALE * LOG2E;
constexpr float MSHIFT = 40.0f;
constexpr u32 SPIN_CAP = 1u << 17;
constexpr u32 SENT = 0xFFFFFFFFu;

DI u32 pk(float lo, float hi) { f32x2 v = {lo, hi}; return __builtin_bit_cast(u32, __builtin_convertvector(v, bf2_t)); }
DI u32x4 pk8(const f32x16& a, int b) { u32x4 r = {pk(a[b], a[b + 1]), pk(a[b + 2], a[b + 3]), pk(a[b + 4], a[b + 5]), pk(a[b + 6], a[b + 7])}; return r; }
DI float blo(u32 x) { return __uint_as_float(x << 16); }
DI float bhi(u32 x) { return __uint_as_float(x & 0xffff0000u); }
DI float sel(bool c, float a, float b) { PIN(a); PIN(b); return c ? a : b; }
DI float swapmax(float m) { auto r = __builtin_amdgcn_permlane32_swap(__float_as_uint(m), __float_as_uint(m), false, false); return fmaxf(__uint_as_float(r[0]), __uint_as_float(r[1])); }
DI float swapadd(float m) { auto r = __builtin_amdgcn_permlane32_swap(__float_as_uint(m), __float_as_uint(m), false, false); return __uint_as_float(r[0]) + __uint_as_float(r[1]); }
#define NTS ""
DI void glds16(u32 voff, const void* sbase, u32 ldsaddr) {
  u32 keep;
  asm volatile("s_mov_b32 %0, m0\n\ts_mov_b32 m0, %3\n\ts_nop 0\n\tglobal_load_lds_dwordx4 %1, %2" NTS "\n\ts_mov_b32 m0, %0"
               : "=&s"(keep) : "v"(voff), "s"(sbase), "s"(ldsaddr) : "memory");
}
DI void glds16_sc1(u32 voff, const void* sbase, u32 ldsaddr) {
  u32 keep;
  asm volatile("s_mov_b32 %0, m0\n\ts_mov_b32 m0, %3\n\ts_nop 0\n\tglobal_load_lds_dwordx4 %1, %2 sc1\n\ts_mov_b32 m0, %0"
               : "=&s"(keep) : "v"(voff), "s"(sbase), "s"(ldsaddr) : "memory");
}
DI void touch_l2(u32 voff, const void* sbase, u32 ldsaddr) {
  u32 keep;
  asm volatile("s_mov_b32 %0, m0\n\ts_mov_b32 m0, %3\n\ts_nop 0\n\tglobal_load_lds_dword %1, %2" NTS "\n\ts_mov_b32 m0, %0"
               : "=&s"(keep) : "v"(voff), "s"(sbase), "s"(ldsaddr) : "memory");
}
DI void glds63(u32 voff, const void* sbase, u32 ldsaddr) {
  u32 keep;
  asm volatile("s_mov_b32 %0, m0\n\ts_mov_b32 m0, %3\n\ts_bitset0_b32 exec_hi, 31\n\tglobal_load_lds_dwordx4 %1, %2\n\ts_bitset1_b32 exec_hi, 31\n\ts_mov_b32 m0, %0"
               : "=&s"(keep) : "v"(voff), "s"(sbase), "s"(ldsaddr) : "memory");
}
DI u32x4 ldsr128(const LAS char* p) { return *(const LAS u32x4*)p; }
DI void ldsw128(LAS char* p, u32x4 v) { *(LAS u32x4*)p = v; }
typedef short v4i16_t __attribute__((ext_vector_type(4)));
DI u32x2 ldstr(const LAS char* p) { return __builtin_bit_cast(u32x2, __builtin_amdgcn_ds_read_tr16_b64_v4i16((LAS v4i16_t*)p)); }
DI u32x4 trfrag(const LAS char* p0, const LAS char* p1) { u32x2 a = ldstr(p0), b = ldstr(p1); u32x4 r = {a[0], a[1], b[0], b[1]}; return r; }
#define LBAR() do { __builtin_amdgcn_s_waitcnt(0xC07F); asm volatile("s_barrier" ::: "memory"); SBAR(); } while (0)

DI void lds_set(LAS char* p) { asm volatile("" ::: "memory"); __hip_atomic_store((LAS u32*)p, 1u, __ATOMIC_RELAXED, __HIP_MEMORY_SCOPE_WORKGROUP); asm volatile("" ::: "memory"); }
DI void lds_wait(const LAS char* p, u32 want) {
#pragma unroll 1
  for (u32 spins = 0; spins < (SPIN_CAP << 4); ++spins) {
    const u32 v = __hip_atomic_load((const LAS u32*)p, __ATOMIC_RELAXED, __HIP_MEMORY_SCOPE_WORKGROUP);
    if (__all(v == want)) break;
    __builtin_amdgcn_s_sleep(1);
  }
  asm volatile("" ::: "memory");
}
DI void wait_flags(const char* ws, u32 laneoff, u64 tag) {
  const GAS u64* f = (const GAS u64*)(ws + laneoff);
#pragma unroll 1
  for (u32 spins = 0; spins < SPIN_CAP; ++spins) {
    u64 v = __hip_atomic_load(f, RLX_AGENT);
    if (__all(v == tag)) break;
    __builtin_amdgcn_s_sleep(POLL_SLEEP);
  }
  __builtin_amdgcn_fence(__ATOMIC_ACQUIRE, "wavefront");
  asm volatile("" ::: "memory");
}
DI void set_flag(char* ws, u32 off, u64 tag) { __hip_atomic_store((GAS u64*)(ws + off), tag, RLX_AGENT); }

#define ST(i) do {} while (0)

#define TP(i) do {} while (0)
#define LWAIT(N, c) asm volatile("s_waitcnt vmcnt(" #N ")\n\ts_barrier" ::: "memory")
DI void lds_add1(LAS char* p, int lane) { asm volatile("" ::: "memory"); if (lane == 0) __hip_atomic_fetch_add((LAS u32*)p, 1u, __ATOMIC_RELAXED, __HIP_MEMORY_SCOPE_WORKGROUP); asm volatile("" ::: "memory"); }
#define SLOTADDR(c) do { const u32 sb_ = (u32)((c) & 3) * SLOT_B; xa = sb_ + xa0; xh = sb_ + xh0; wp = lds + sb_ + wa0; } while (0)
#define RDX(j) x[j] = ldsr128(lds + (xa ^ ((j) << 5)))
#define RDH(j) do { if (HALO) hx[j] = ldsr128(lds + (xh ^ ((j) << 5))); } while (0)
#define RDW(t, j) f[t][j] = PACKED ? ldsr128(wp + (j) * 3072 + (t) * 1024) : trfrag(wp + (j) * 3072 + (t) * 56, wp + (j) * 3072 + 768 + (t) * 56)
#define MM(A, t, j) A = ((t) >= 3 - NV) ? MFMA(x[j], f[t][j], A) : MFMA(f[t][j], x[j], A)
#define MH(j) do { if (HALO == 1) ah = MFMA(f[1][j], hx[j], ah); if (HALO == 2) ah = MFMA(hx[j], f[2][j], ah); } while (0)
#define HALF(m, r, I0, I1, I2, I3) do { \
    MM(a0, 0, m); SBAR(); RDX(r); RDW(0, r); SBAR(); \
    MM(a1, 1, m); SBAR(); RDW(1, r); I0; SBAR(); \
    MM(a2, 2, m); SBAR(); RDW(2, r); RDH(r); I1; SBAR(); \
    MH(m); SBAR(); \
    MM(a0, 0, m + 1); SBAR(); RDX(r + 1); RDW(0, r + 1); I2; SBAR(); \
    MM(a1, 1, m + 1); SBAR(); RDW(1, r + 1); RDW(2, r + 1); RDH(r + 1); SBAR(); \
    MM(a2, 2, m + 1); SBAR(); I3; SBAR(); \
    MH(m + 1); SBAR(); } while (0)
#define RDFIRST() do { RDX(0); RDW(0, 0); RDW(1, 0); RDW(2, 0); RDH(0); RDX(1); RDW(0, 1); RDW(1, 1); RDW(2, 1); RDH(1); SBAR(); } while (0)

template <int NV>
DI void gemmB(LAS char* lds, u32 xa0, u32 wa0, f32x16& a0, f32x16& a1, f32x16& a2, const char* ws, int fstep, u32 flagoff, u64 tag, int lane) {
  constexpr bool PACKED = false; constexpr int HALO = 0;
  u32x4 x[4], f[3][4], hx[4]; f32x16 ah;
  u32 xa, xh; const u32 xh0 = 0; const LAS char* wp;
  LBAR();
  SLOTADDR(0);
  RDFIRST();
#pragma unroll 1
  for (int c = 0; c < 7; ++c) {
    HALF(0, 2, , , , );
    asm volatile("s_cmp_eq_u32 %0, %1\n\ts_cbranch_scc0 1f\n\ts_waitcnt vmcnt(0)\n\ts_mov_b64 exec, 3\n\tglobal_store_dwordx2 %2, %3, %4 sc1\n\ts_mov_b64 exec, -1\n1:"
                 :: "s"(c), "s"(fstep), "v"(flagoff), "v"(tag), "s"(ws) : "memory", "scc");
    __builtin_amdgcn_s_waitcnt(0xC07F);
    asm volatile("s_cmp_eq_u32 %0, 6\n\ts_cbranch_scc1 1f\n\ts_barrier\n1:" :: "s"(c) : "memory", "scc");
    SBAR();
    SLOTADDR(c + 1);
    HALF(2, 0, , , , );
  }
}

template <int HALO>
DI void gemmA(LAS char* lds, u32 xa0, u32 xh0, u32 wa0, f32x16& a0, f32x16& a1, f32x16& a2, f32x16& ah) {
  constexpr bool PACKED = true; constexpr int NV = 1;
  u32x4 x[4], f[3][4], hx[4];
  u32 xa, xh; const LAS char* wp;
  SLOTADDR(7);
  RDFIRST();
#pragma unroll 1
  for (int c = 7; c < 14; ++c) {
    HALF(0, 2, , , , );
    __builtin_amdgcn_s_waitcnt(0xC07F);
    asm volatile("s_waitcnt vmcnt(0)\n\ts_cmp_eq_u32 %0, 13\n\ts_cbranch_scc1 1f\n\ts_barrier\n1:" :: "s"(c) : "memory", "scc");
    SBAR();
    SLOTADDR(c + 1);
    HALF(2, 0, , , , );
  }
}

__global__ __launch_bounds__(512, 2) void fused_attn(const char* __restrict__ Ax, const char* __restrict__ Aw, const char* __restrict__ Ao,
                                                     const char* __restrict__ Bx, const char* __restrict__ Bw, const char* __restrict__ Bo,
                                                     char* __restrict__ out, char* ws) {
  __shared__ __attribute__((aligned(16))) char lds_raw[LDS_BYTES];
  LAS char* lds = (LAS char*)lds_raw;
  const u32 lds0 = (u32)(size_t)lds;
  const int tid = threadIdx.x, lane = tid & 63, r32 = lane & 31, hh = lane >> 5;
  const int w = __builtin_amdgcn_readfirstlane(tid >> 6);
  const int bid = blockIdx.x, xcd = bid & 7, slot = bid >> 3;
  const int rb = 4 * (xcd & 3) + (slot & 3), g = 8 * (xcd >> 2) + (slot >> 2);
  const int h = 2 * xcd + (slot >> 4), qb = slot & 15;
  const int cg = slot & 7, ot0 = 8 * xcd + 2 * (slot >> 3);
  const int tq = w & 3;
  asm volatile("" :: "s"(Ax), "s"(Aw), "s"(Ao), "s"(Bx), "s"(Bw), "s"(Bo), "s"(out), "s"(ws));
  ST(0);
  u64 stv = 0;
  const bool packer = w < 3 && rb < 14;
  if (packer) {
    const u32 vo = (u32)(32 * rb + (lane >> 2)) * 2688 + tq * 896 + 56 * g - 8 * (g & 1) + (lane & 3) * 16;
    glds16(vo, Aw, lds0 + L_X + tq * 2560); glds16(vo, Aw + 16 * 2688, lds0 + L_X + tq * 2560 + 1024);
  }
  int kc = 2 * ((g + rb) & 3);
  const int p0 = (tq & 1) + 8 * (tq >> 1);
  const u32 voffX = (u32)(128 * rb + 8 * p0 + (lane >> 3)) * 896u + (u32)(((lane & 7) ^ ((4 * (tq & 1) + (lane >> 4)) & 7)) << 4);
  { const int j0 = (w >> 2) * 2;
    glds16(voffX, Bx + kc * 128 + j0 * 14336, lds0 + (p0 + 2 * j0) * 1024);
    glds16(voffX, Bx + kc * 128 + (j0 + 1) * 14336, lds0 + (p0 + 2 * j0 + 2) * 1024); }
  u32 voffW[3];
#pragma unroll
  for (int j = 0; j < 3; ++j) { const int i = 64 * j + lane, kr = (i * 171) >> 11, rem = i - kr * 12; voffW[j] = (16 * tq + kr) * 2688 + 168 * g - 8 * (g & 1) + min(rem, 10) * 16; }
  if (w >= 4) { glds16(voffW[0], Bw + kc * (64 * 2688), lds0 + SLOT_W + (3 * tq) * 1024); glds16(voffW[1], Bw + kc * (64 * 2688), lds0 + SLOT_W + (3 * tq + 1) * 1024); }
  else glds16(voffW[2], Bw + kc * (64 * 2688), lds0 + SLOT_W + (3 * tq + 2) * 1024);
  kc = kc == 6 ? 0 : kc + 1;

  if (w == 3) {
    u64 pc; u32 len;
    asm volatile("s_getpc_b64 %0\n.Lfa_pc:\n\ts_mov_b32 %1, .Lfa_end-.Lfa_pc" : "=s"(pc), "=s"(len));
    touch_l2(min((u32)(slot + 32 * (lane & 15)) * 128u, len), (const void*)pc, lds0 + L_SYNC + 1024);
  }
  const __amdgpu_buffer_rsrc_t rs = __builtin_amdgcn_make_buffer_rsrc(ws, 0, WS_END, 0x00020000);
  const f32x16 zero16 = {};
  u64 tag;
  u32x4 qf[4][2];
  u32x4 kv[8][4];
  const u32 kvb = WS_T + (u32)(((16 + h) * 64 + 8 * w) * 2) * 1024 + lane * 16;
  const int trot = qb >> 1;
  const int ptile = 4 * rb + tq;
#define LOADF(t, f) kv[t][f] = BLD(kvb + ((f) & 1) * 1024, (((t) + trot) & 7) * 2048 + ((f) >> 1) * 2097152)
#define LOADT(t) do { LOADF(t, 0); LOADF(t, 1); LOADF(t, 2); LOADF(t, 3); } while (0)
#define READQ() do { _Pragma("unroll") for (int i_ = 0; i_ < 4; ++i_) { const u32 a_ = L_Q + (u32)((i_ ^ (w >> 1)) * 2048) + lane * 16; \
      qf[i_][0] = ldsr128(lds + a_); qf[i_][1] = ldsr128(lds + a_ + 1024); } } while (0)
#define GETTAG() do { const u64 gen_ = *(const __attribute__((address_space(4))) u64*)(ws + WS_GEN); \
      tag = gen_ * 6364136223846793005ull + 1442695040888963407ull; asm volatile("" : "+s"(tag)); } while (0)
  u32 voffH;
  { const int hr = 8 * (tq & 1) + (lane >> 3); int tok = 128 * rb - 16 + hr; tok = tok < 0 ? 0 : tok;
    voffH = (u32)tok * 896u + (u32)(((lane & 7) ^ ((hr >> 1) & 7)) << 4); }
  auto issueA = [&](int cc) {
    const u32 sb = lds0 + (u32)(cc & 3) * SLOT_B;
    const char* xs = Ax + kc * 128; const char* wq = ws + WS_P + g * 86016 + kc * 12288 + tq * 3072;
    kc = kc == 6 ? 0 : kc + 1;
#pragma unroll
    for (int j = 0; j < 4; ++j) glds16(voffX, xs + j * 14336, sb + (p0 + 2 * j) * 1024);
    if (tq < 2) glds16(voffH, xs, sb + SLOT_HALO + tq * 1024);
#pragma unroll
    for (int j = 0; j < 3; ++j) glds16_sc1((u32)lane * 16, wq + j * 1024, sb + SLOT_W + (3 * tq + j) * 1024);
  };
  const int xrow = 32 * tq + r32;
  const u32 xa0 = xrow * 128 + ((hh ^ ((xrow >> 1) & 7)) << 4);
  const u32 xh0 = SLOT_HALO + (r32 & 15) * 128 + ((hh ^ (((r32 & 15) >> 1) & 7)) << 4);
  f32x16 aq = zero16, ak = zero16, av = zero16, ah = zero16;

  if (w >= 4) {
    auto issueB = [&](int cc) {
      const u32 sb = lds0 + (u32)(cc & 3) * SLOT_B;
      const char* xs = Bx + kc * 128; const char* wq = Bw + kc * (64 * 2688);
      kc = kc == 6 ? 0 : kc + 1;
#pragma unroll
      for (int j = 0; j < 4; ++j) {
        glds16(voffX, xs + j * 14336, sb + (p0 + 2 * j) * 1024);
        if (j < 3) glds16(voffW[j], wq, sb + SLOT_W + (3 * tq + j) * 1024);
      }
    };
#define XTOUCH() do { int row0 = 128 * rb - 16 + 18 * (g & 7); row0 = row0 < 0 ? 0 : row0; \
      touch_l2((u32)row0 * 896u + (u32)min(tq * 32 + r32, 125) * 128u, Ax, lds0 + L_SYNC + 1024); } while (0)
    issueB(1);
    if (XPF_STEP < 0) XTOUCH();
    GETTAG();
    const u32 pflag = WS_FP + (u32)(g * 64 + (lane < 42 ? lane : 0)) * 8;
    u64 pfv = 0;
#pragma unroll 1
    for (int c = 0; c < 5; ++c) {
      { u32 t_; asm volatile("s_sub_u32 %0, %1, %2\n\ts_cmp_lt_u32 %0, 2\n\ts_cbranch_scc1 1f\n\ts_waitcnt vmcnt(7)\n1:" : "=&s"(t_) : "s"(c), "n"(XPF_STEP + 1) : "memory", "scc"); }
      LWAIT(8, c);
      asm volatile("s_cmp_eq_u32 %3, %4\n\ts_cbranch_scc0 1f\n\tglobal_load_dwordx2 %0, %1, %2 sc1\n1:" : "+v"(pfv) : "v"(pflag), "s"(ws), "s"(c), "s"(w) : "memory", "scc");
      issueB(c + 2);
      if (c == XPF_STEP) XTOUCH();
    }
    asm volatile("s_waitcnt vmcnt(7)" : "+v"(pfv) :: "memory");
    if (w == 4 && !__all(pfv == tag)) wait_flags(ws, pflag, tag);
    LWAIT(7, 5);
    issueA(7);
    LWAIT(7, 6);
    issueA(8);
    ST(1);
    asm volatile("s_waitcnt vmcnt(7)\n\ts_barrier" ::: "memory");
    { const u32 wp0 = SLOT_W + lane * 16;
      if (tq == 2) gemmA<1>(lds, xa0, xh0, wp0, aq, ak, av, ah);
      else if (tq == 3) gemmA<2>(lds, xa0, xh0, wp0, aq, ak, av, ah);
      else gemmA<0>(lds, xa0, xh0, wp0, aq, ak, av, ah); }
    ST(2);
    TP(60);
    __builtin_amdgcn_s_setprio(3);
    LAS char* xb = lds + L_X;
    u32x4 aq0 = pk8(aq, 0), aq1 = pk8(aq, 8), ak0 = pk8(ak, 0), ak1 = pk8(ak, 8), cv0 = pk8(av, 0), cv1 = pk8(av, 8);
    if (hh) { aq1[2] = 0; aq1[3] = 0; ak1[2] = 0; ak1[3] = 0; }
    if (r32 >= 28) { cv0 = (u32x4){0, 0, 0, 0}; cv1 = cv0; }
    if (tq < 3) { LAS char* p = xb + (tq + 1) * 3072 + lane * 16; ldsw128(p, ak0); ldsw128(p + 1024, ak1); ldsw128(p + 2048, cv1); }
    if (tq == 2) {
      u32x4 h0 = pk8(ah, 0), h1 = pk8(ah, 8);
      if (hh) { h1[2] = 0; h1[3] = 0; }
      ldsw128(xb + lane * 16, h0); ldsw128(xb + 1024 + lane * 16, h1);
    } else if (tq == 3) {
      u32x4 h1 = pk8(ah, 8);
      if (r32 >= 28) h1 = (u32x4){0, 0, 0, 0};
      ldsw128(xb + 2048 + lane * 16, h1);
    }
    lds_set(lds + L_SYNC + 16 + tq * 4);
    f32x16 sc = MFMA(ak0, aq0, zero16); sc = MFMA(ak1, aq1, sc);
    lds_wait(lds + L_SYNC + 16 + (lane & 3) * 4, 1);
    u32x4 pk0 = ldsr128(xb + tq * 3072 + lane * 16), pk1 = ldsr128(xb + tq * 3072 + 1024 + lane * 16), pv1 = ldsr128(xb + tq * 3072 + 2048 + lane * 16);
    f32x16 sp = MFMA(pk0, aq0, zero16); sp = MFMA(pk1, aq1, sp);
    const bool odd = r32 & 1; const bool pvalid = ptile != 0;
    float t[12];
    float mx = -INFINITY;
#pragma unroll
    for (int i = 0; i < 12; ++i) {
      const int rp = i < 8 ? 2 * i : 2 * i - 8;
      const float sv = i < 8 ? sel(odd, sc[rp + 1], sc[rp]) : sel(odd, sp[rp + 1], sp[rp]);
      const int rho = (rp & 3) + 8 * (rp >> 2) + 4 * hh;
      const int dl = (r32 & ~1) - rho + (i < 8 ? 0 : 32);
      const bool ok = (unsigned)dl <= 14u && (i < 8 || pvalid);
      const float l1 = blo(pk(sv, 0.f)) * SCALE;
      const float l2 = blo(pk(l1, 0.f));
      t[i] = ok ? l2 : -INFINITY;
      mx = fmaxf(mx, t[i]);
    }
    mx = swapmax(mx);
    float sum = 0.f;
#pragma unroll
    for (int i = 0; i < 12; ++i) { t[i] = __builtin_amdgcn_exp2f((t[i] - mx) * LOG2E); sum += t[i]; }
    sum = swapadd(sum);
    const float inv = __builtin_amdgcn_rcpf(sum);
    u32 pw[12];
#pragma unroll
    for (int i = 0; i < 12; ++i) { const u32 b = pk(t[i] * inv, 0.f); pw[i] = odd ? (b << 16) : b; }
    const u32x4 p0 = {pw[0], pw[1], pw[2], pw[3]}, p1 = {pw[4], pw[5], pw[6], pw[7]}, pp = {pw[8], pw[9], pw[10], pw[11]};
    f32x16 oa = MFMA(cv0, p0, zero16); oa = MFMA(cv1, p1, oa); oa = MFMA(pv1, pp, oa);
    BST(pk8(oa, 0), WS_O + (u32)(ptile * 16 + g) * 2048 + lane * 16);
    BST(pk8(oa, 8), WS_O + (u32)(ptile * 16 + g) * 2048 + 1024 + lane * 16);
    lds_wait(lds + L_SYNC + (lane & 3) * 4, 1);
    LOADT(0); LOADT(1);
    READQ();
  } else {
    if (tid < 16) *(LAS u32*)(lds + L_SYNC + tid * 4) = 0;
    GETTAG();
    asm volatile("s_waitcnt vmcnt(3)" ::: "memory");
    if (packer) {
      const LAS char* pp = lds + L_X + tq * 2560 + (8 * hh + ((lane & 15) >> 2)) * 64 + 8 * (g & 1) + ((lane >> 4) & 1) * 32 + (lane & 3) * 8;
      const u32x4 f0 = trfrag(pp, pp + 256), f1 = trfrag(pp + 1024, pp + 1280);
      const u32 po = WS_P + (u32)(g * 84 + 6 * rb + tq) * 1024 + lane * 16;
      BST(f0, po); BST(f1, po + 3072);
      asm volatile("s_waitcnt vmcnt(2)" ::: "memory");
    } else asm volatile("s_waitcnt vmcnt(0)" ::: "memory");
    { const u32 wa0 = SLOT_W + (8 * hh + ((lane & 15) >> 2)) * 192 + 8 * (g & 1) + ((lane >> 4) & 1) * 32 + (lane & 3) * 8;
      const int fs = (packer || w == 0) ? PFLAG_STEP : 99;
      const u32 fo = (packer && !(w == 0 && lane == 1)) ? WS_FP + (u32)(g * 64 + 3 * rb + tq) * 8 : WS_FST + (u32)bid * 8;
      if (g < 10) gemmB<0>(lds, xa0, wa0, aq, ak, av, ws, fs, fo, tag, lane);
      else if (g == 10) gemmB<1>(lds, xa0, wa0, aq, ak, av, ws, fs, fo, tag, lane);
      else gemmB<3>(lds, xa0, wa0, aq, ak, av, ws, fs, fo, tag, lane); }
    ST(1);
    __builtin_amdgcn_s_setprio(3);
    const u32 to = WS_T + (u32)((3 * g * 64 + ptile) * 2) * 1024 + lane * 16;
    auto put = [&](f32x16& a, int j) {
      const int sl = 3 * g + j;
      if (sl < 16) a *= QSC;
      u32x4 t0 = pk8(a, 0), t1 = pk8(a, 8);
      if (sl < 32) { if (hh) { t1[2] = sl < 16 ? 0u : 0x00003F80u; t1[3] = 0; } }
      else if (r32 >= 28) { const u32 f = r32 == 28 ? 0x3F803F80u : 0u; t0 = (u32x4){f, f, f, f}; t1 = t0; }
      BST(t0, to + j * 131072); BST(t1, to + j * 131072 + 1024);
    };
    kc = kc == 6 ? 0 : kc + 1;
    __builtin_amdgcn_s_waitcnt(0xC07F);
    LWAIT(63, 7);
    put(aq, 0); put(ak, 1); put(av, 2);
    LWAIT(63, 8);
    issueA(9);
    asm volatile("s_waitcnt vmcnt(7)" ::: "memory");
    u32 prevs;
    { u32 prev = 0;
      if (lane == 0) prev = __hip_atomic_fetch_add((LAS u32*)(lds + L_SYNC + 40), 1u, __ATOMIC_RELAXED, __HIP_MEMORY_SCOPE_WORKGROUP);
      prevs = __builtin_amdgcn_readfirstlane(prev);
      if (prevs == 3 && lane < 3) set_flag(ws, WS_FT + (u32)((3 * g + lane) * 16 + rb) * 8, tag); }
    issueA(10);
    const u32 tflag = WS_FT + (u32)((lane & 32) ? h * 16 + qb : (16 + (lane & 16) + h) * 16 + (lane & 15)) * 8;
    u64 tfv = 0;
    asm volatile("s_cmp_eq_u32 %0, 3\n\ts_cbranch_scc1 1f\n\ts_waitcnt vmcnt(7)\n1:" :: "s"(prevs) : "memory", "scc");
    LWAIT(8, 9);  issueA(11);
    LWAIT(7, 10); issueA(12);
    LWAIT(7, 11); issueA(13);
    LWAIT(7, 12);
    asm volatile("s_cmp_eq_u32 %3, 0\n\ts_cbranch_scc0 1f\n\tglobal_load_dwordx2 %0, %1, %2 sc1\n\ts_waitcnt vmcnt(1)\n\ts_branch 2f\n1:\n\ts_waitcnt vmcnt(0)\n2:"
                 : "+v"(tfv) : "v"(tflag), "s"(ws), "s"(w) : "memory", "scc");
    LWAIT(1, 13);
    TP(28);
    if (w == 0) {
      asm volatile("s_waitcnt vmcnt(0)" : "+v"(tfv) :: "memory");
      if (!__all(tfv == tag)) wait_flags(ws, tflag, tag);
      lds_set(lds + L_SYNC + 44);
    } else lds_wait(lds + L_SYNC + 44, 1);
    TP(29);
    u32x4 q0 = BLD(WS_T + (u32)((h * 64 + 4 * qb + tq) * 2) * 1024 + lane * 16, 0), q1 = BLD(WS_T + (u32)((h * 64 + 4 * qb + tq) * 2) * 1024 + lane * 16, 1024);
    LOADT(0); LOADT(1);
    SBAR();
    f32x16 s = MFMA(kv[0][0], q0, zero16); s = MFMA(kv[0][1], q1, s);
    ldsw128(lds + L_Q + tq * 2048 + lane * 16, q0);
    float m = s[0];
#pragma unroll
    for (int i = 1; i < 16; ++i) m = fmaxf(m, s[i]);
    m = swapmax(m);
    if (hh) q1[2] = pk(-(m + MSHIFT), 0.f);
    ldsw128(lds + L_Q + tq * 2048 + 1024 + lane * 16, q1);
    lds_set(lds + L_SYNC + tq * 4);
    ST(2);
    TP(30);
    lds_wait(lds + L_SYNC + (lane & 3) * 4, 1);
    READQ();
    TP(31);
  }
  __builtin_amdgcn_s_waitcnt(0xC07F);
  if (lane == 0) __hip_atomic_fetch_add((LAS u32*)(lds + L_SYNC + 32), 1u, __ATOMIC_RELAXED, __HIP_MEMORY_SCOPE_WORKGROUP);
  ST(3);
  const int qt2 = w & 1, kq = w >> 1, otile = ot0 + qt2;
  const u32 ofo = (u32)(otile * 16 + 4 * kq) * 2048 + lane * 16;
  u32x4 of[8];
  f32x16 o[4];
  {
    const u32 voffO = (u32)((lane / 7) * 896 + (lane % 7) * 16 + cg * 112);
    const int wr6 = w < 2 ? (w == 0 ? 432 : 439) : 9 * w + 360;
#define WOP(j) do { const int jj_ = (j) % 7, r_ = jj_ < 6 ? 9 * w + 72 * jj_ : wr6; \
      glds63(voffO, ((j) >= 7 ? Bo : Ao) + r_ * 896, lds0 + ((j) >= 7 ? WO_B : 0) + r_ * 112); } while (0)
    const u32 fao = WS_FO + (u32)(lane & 4) * 2048 + (u32)(otile * 16 + 4 * kq + (lane & 3)) * 8;
    const GAS u64* fa = (const GAS u64*)(ws + fao);
    u64 fav;
    f32x16 s[2]; u32x4 pa, pb;
    __builtin_amdgcn_s_setprio(2);
    SBAR();
    s[0] = MFMA(kv[0][0], qf[0][0], zero16); s[0] = MFMA(kv[0][1], qf[0][1], s[0]);
    SBAR();
#define EXP(x) x = __builtin_amdgcn_exp2f(x)
#define STEP(u) do { \
      constexpr int kt = (u) >> 2, qt = (u) & 3, un = (u) + 1, ktn = (un >> 2) & 7, qtn = un & 3, up = (u) > 0 ? (u) - 1 : 0, kl = (kt + 3) & 7; \
      f32x16& sc = s[(u) & 1]; f32x16& sn = s[un & 1]; \
      if (un < 32) { sn = MFMA(kv[ktn][0], qf[qtn][0], zero16); PIN(sn); } SBAR(); \
      if ((u) < 4) LOADF(2, qt); \
      if (kt + 3 < 8) LOADF(kl, qt); \
      if ((u) >= 24) of[(u) & 7] = BLD(WS_O + ofo, ((u) & 7) * 1024); \
      EXP(sc[0]); EXP(sc[1]); EXP(sc[2]); EXP(sc[3]); PIN(sc); SBAR(); \
      if ((u) > 0) { o[up & 3] = MFMA(kv[up >> 2][3], pb, o[up & 3]); PIN(o[up & 3]); } SBAR(); \
      EXP(sc[4]); EXP(sc[5]); PIN(sc); SBAR(); pa[0] = pk(sc[0], sc[1]); pa[1] = pk(sc[2], sc[3]); PIN(pa); SBAR(); EXP(sc[6]); EXP(sc[7]); PIN(sc); SBAR(); \
      if (un < 32) { sn = MFMA(kv[ktn][1], qf[qtn][1], sn); PIN(sn); } SBAR(); \
      if ((u) >= 12 && (u) < 26) WOP((u) - 12); \
      EXP(sc[8]); PIN(sc); SBAR(); pa[2] = pk(sc[4], sc[5]); pa[3] = pk(sc[6], sc[7]); PIN(pa); SBAR(); EXP(sc[9]); EXP(sc[10]); EXP(sc[11]); PIN(sc); SBAR(); \
      o[qt] = MFMA(kv[kt][2], pa, (u) < 4 ? zero16 : o[qt]); PIN(o[qt]); SBAR(); \
      EXP(sc[12]); EXP(sc[13]); EXP(sc[14]); EXP(sc[15]); PIN(sc); SBAR(); \
      pb[0] = pk(sc[8], sc[9]); pb[1] = pk(sc[10], sc[11]); pb[2] = pk(sc[12], sc[13]); pb[3] = pk(sc[14], sc[15]); PIN(pb); SBAR(); \
      if ((u) == 15) __builtin_amdgcn_s_setprio(1); \
      if ((u) == 26) __builtin_amdgcn_s_setprio(0); \
    } while (0)
#define STEP4(t) STEP(4 * (t)); STEP(4 * (t) + 1); STEP(4 * (t) + 2); STEP(4 * (t) + 3)
    STEP4(0);
    touch_l2((u32)(slot * 98 + min((w & 3) * 32 + r32, 97)) * 128u, w < 4 ? Ao : Bo, lds0 + L_SYNC + 1024);
    {
      const u32x4 sv = {SENT, SENT, SENT, SENT};
      asm volatile("s_cmp_lt_u32 %0, 4\n\ts_cbranch_scc0 1f\n\tbuffer_store_dwordx4 %1, %2, %3, 0 offen sc1\n\tbuffer_store_dwordx4 %1, %2, %3, 0 offen offset:1024 sc1\n\ts_nop 1\n1:"
                   :: "s"(w), "v"(sv), "v"(WS_O + 2097152 + (u32)((4 * qb + tq) * 16 + h) * 2048 + lane * 16), "s"(rs) : "memory", "scc"); }
    STEP4(1); STEP4(2);
    lds_wait(lds + L_SYNC + 16 + (lane & 3) * 4, 1);
    asm volatile("s_waitcnt vmcnt(8)\n\ts_mov_b64 exec, 1\n\tglobal_store_dwordx2 %0, %1, %2 sc1\n\ts_mov_b64 exec, -1"
                 :: "v"(w < 4 ? WS_FS + (u32)((4 * qb + tq) * 16 + h) * 8 : WS_FO + (u32)(ptile * 16 + g) * 8), "v"(tag), "s"(ws) : "memory");
    if (bid == 0 && w == 0) {
#pragma unroll 1
      for (u32 spins = 0; spins < SPIN_CAP; ++spins) {
        const u32x4 fa0 = BLD(WS_FST + lane * 32, 0), fa1 = BLD(WS_FST + lane * 32, 16);
        const u32 tl = (u32)tag, th = (u32)(tag >> 32);
        if (__all(fa0[0] == tl && fa0[1] == th && fa0[2] == tl && fa0[3] == th && fa1[0] == tl && fa1[1] == th && fa1[2] == tl && fa1[3] == th)) break;
        __builtin_amdgcn_s_sleep(4);
      }
      if (lane == 0) *(GAS u64*)(ws + WS_GEN) = tag;
    }
    STEP4(3); STEP4(4);
    fav = __hip_atomic_load(fa, RLX_AGENT);
    SBAR();
    STEP4(5);
    if (__builtin_expect(!__all(fav == tag), 0)) wait_flags(ws, fao, tag);
    asm volatile("" ::: "memory");
    STEP4(6);
    lds_wait(lds + L_SYNC + 32, 8);
    STEP4(7);
    o[3] = MFMA(kv[7][3], pb, o[3]);
  }
  ST(4);
  const u32 wob = (u32)(kq * 4 * 3136 + ((lane & 15) >> 2) * 112 + ((lane >> 4) & 1) * 32 + (lane & 3) * 8);
  u32x4 wf[16];
#define RDWO(br, i) do { const LAS char* wb_ = lds + L_WO + (br) * WO_B + wob + (i) * 3136; const LAS char* wa_ = wb_ + hh * 448; \
    wf[4 * (i) + 0] = trfrag(wa_, wa_ + 896); wf[4 * (i) + 1] = trfrag(wa_ + 56, wa_ + 56 + 896); \
    wf[4 * (i) + 2] = trfrag(wa_ + 1792, wb_ + 2688); wf[4 * (i) + 3] = trfrag(wa_ + 56 + 1792, wb_ + 56 + 2688); } while (0)
  {
    const int ws1 = w & 1;
    { const u32x4 a = pk8(o[0], 0), b = pk8(o[0], 8); const u32x4 oth = ws1 ? a : b;
      ldsw128(lds + L_SCR + (u32)((w ^ 1) * 7 + 0) * 1024 + lane * 16, oth); }
#pragma unroll
    for (int i = 1; i < 4; ++i)
#pragma unroll
      for (int s2 = 0; s2 < 2; ++s2) {
        const int owner = ((i ^ (w >> 1)) << 1) | s2;
        ldsw128(lds + L_SCR + (u32)(owner * 7 + (w ^ owner) - 1) * 1024 + lane * 16, pk8(o[i], 8 * s2));
      }
    if (hh) {
#pragma unroll
      for (int i = 0; i < 4; ++i) *(LAS float*)(lds + L_LP + (u32)(((i ^ (w >> 1)) * 32 + r32) * 32 + w * 4)) = o[i][12];
    }
    f32x16 acc = o[0];
#pragma unroll
    for (int j = 0; j < 8; ++j) acc[j] = sel(ws1, o[0][8 + j], o[0][j]);
    u32x4 idf;
    { const int jj = r32 - 4 * hh;
      idf[0] = jj == 0 ? 0x3F80u : jj == 1 ? 0x3F800000u : 0u; idf[1] = jj == 2 ? 0x3F80u : jj == 3 ? 0x3F800000u : 0u;
      idf[2] = jj == 8 ? 0x3F80u : jj == 9 ? 0x3F800000u : 0u; idf[3] = jj == 10 ? 0x3F80u : jj == 11 ? 0x3F800000u : 0u; }
    PIN(acc); PIN(idf);
    asm volatile("s_waitcnt vmcnt(6)" ::: "memory");
    LBAR();
    ST(5);
    u32x4 pv[7];
#pragma unroll
    for (int k = 0; k < 7; ++k) pv[k] = ldsr128(lds + L_SCR + (u32)(w * 7 + k) * 1024 + lane * 16);
    const f32x4 la = *(const LAS f32x4*)(lds + L_LP + (u32)(((w >> 1) * 32 + r32) * 32)), lb = *(const LAS f32x4*)(lds + L_LP + (u32)(((w >> 1) * 32 + r32) * 32 + 16));
    SBAR();
#pragma unroll
    for (int k = 0; k < 7; ++k) acc = MFMA(idf, pv[k], acc);
    RDWO(0, 0);
    float l = ((la[0] + la[1]) + (la[2] + la[3])) + ((lb[0] + lb[1]) + (lb[2] + lb[3]));
    float own[8];
#pragma unroll
    for (int j = 0; j < 8; ++j) own[j] = acc[j];
    float chk = 0.f, chk2 = 0.f;
#pragma unroll
    for (int j = 0; j < 4; ++j) { chk = __builtin_fmaf(own[j], 0.f, chk); chk2 = __builtin_fmaf(own[4 + j], 0.f, chk2); }
    chk += chk2;
    const bool good = (l >= 0x1p-70f) && (l <= 0x1p126f) && (chk == 0.f);
    if (__builtin_expect(!__all(good), 0)) {
      u32x4 q0 = qf[0][0], q1 = qf[0][1];
      if (hh) q1[2] = 0;
      const u32 kb = WS_T + (u32)((16 + h) * 64 * 2) * 1024 + lane * 16;
      float m = -INFINITY;
#pragma unroll 1
      for (int t = 0; t < 64; ++t) {
        const u32x4 k0 = BLD(kb, t * 2048), k1 = BLD(kb + 1024, t * 2048);
        f32x16 s = MFMA(k0, q0, zero16); s = MFMA(k1, q1, s);
#pragma unroll
        for (int i = 0; i < 16; ++i) m = fmaxf(m, s[i]);
      }
      m = swapmax(m);
      f32x16 oo = zero16;
#pragma unroll 1
      for (int t = 0; t < 64; ++t) {
        const u32x4 k0 = BLD(kb, t * 2048), k1 = BLD(kb + 1024, t * 2048), v0 = BLD(kb + 2097152, t * 2048), v1 = BLD(kb + 2097152 + 1024, t * 2048);
        f32x16 s = MFMA(k0, q0, zero16); s = MFMA(k1, q1, s);
#pragma unroll
        for (int i = 0; i < 16; ++i) s[i] = __builtin_amdgcn_exp2f(s[i] - m);
        oo = MFMA(v0, pk8(s, 0), oo); oo = MFMA(v1, pk8(s, 8), oo);
      }
      { auto r = __builtin_amdgcn_permlane32_swap(__float_as_uint(oo[12]), __float_as_uint(oo[12]), false, false); l = __uint_as_float(r[1]); }
#pragma unroll
      for (int j = 0; j < 8; ++j) own[j] = sel(ws1, oo[8 + j], oo[j]);
    }
    const float inv = __builtin_amdgcn_rcpf(l);
    u32x4 ob = {pk(own[0] * inv, own[1] * inv), pk(own[2] * inv, own[3] * inv), pk(own[4] * inv, own[5] * inv), pk(own[6] * inv, own[7] * inv)};
    if (ws1 && hh) { ob[2] = 0; ob[3] = 0; }
#pragma unroll
    for (int j = 0; j < 4; ++j) ob[j] = min(ob[j], SENT - 1);
    BST(ob, WS_O + 2097152 + (u32)(((4 * qb + (w >> 1)) * 16 + h) * 2 + ws1) * 1024 + lane * 16);
  }
  ST(6);
  const u32 pwo = (u32)((kq * 64 + qt2 * 32 + r32) * 240 + hh * 16);
  const int trow = tid / 7, tg = tid - trow * 7;
  const u32 pro = (u32)(trow * 240 + tg * 32);
  char* op = out + (size_t)(32 * ot0 + trow) * 1792 + cg * 112 + tg * 16;
  f32x16 c0, c1;
#define MMO(F, i) do { c0 = MFMA(wf[4 * (i) + 0], F[2 * (i)], (i) == 0 ? zero16 : c0); c1 = MFMA(wf[4 * (i) + 1], F[2 * (i)], (i) == 0 ? zero16 : c1); \
    c0 = MFMA(wf[4 * (i) + 2], F[2 * (i) + 1], c0); c1 = MFMA(wf[4 * (i) + 3], F[2 * (i) + 1], c1); } while (0)
#define PUTP(base) do { _Pragma("unroll") for (int g_ = 0; g_ < 4; ++g_) if (g_ < 3 || !hh) { \
      *(LAS f32x4*)(lds + (base) + pwo + g_ * 32) = (f32x4){c0[4 * g_], c0[4 * g_ + 1], c0[4 * g_ + 2], c0[4 * g_ + 3]}; \
      *(LAS f32x4*)(lds + (base) + pwo + 112 + g_ * 32) = (f32x4){c1[4 * g_], c1[4 * g_ + 1], c1[4 * g_ + 2], c1[4 * g_ + 3]}; } } while (0)
#define SUMP(base, br) do { if (w < 7) { const LAS char* p_ = lds + (base) + pro; f32x4 r_[8]; \
      _Pragma("unroll") for (int k_ = 0; k_ < 4; ++k_) { r_[2 * k_] = *(const LAS f32x4*)(p_ + k_ * 15360); r_[2 * k_ + 1] = *(const LAS f32x4*)(p_ + k_ * 15360 + 16); } \
      const f32x4 ra_ = (r_[0] + r_[2]) + (r_[4] + r_[6]), rc_ = (r_[1] + r_[3]) + (r_[5] + r_[7]); \
      *(u32x4*)(op + (br) * 896) = (u32x4){pk(ra_[0], ra_[1]), pk(ra_[2], ra_[3]), pk(rc_[0], rc_[1]), pk(rc_[2], rc_[3])}; } } while (0)
  SBAR();
  RDWO(0, 1); SBAR(); MMO(of, 0); SBAR();
  RDWO(0, 2); SBAR(); MMO(of, 1); SBAR();
  RDWO(0, 3); SBAR(); MMO(of, 2); SBAR();
  MMO(of, 3); SBAR();
  u32x4 og[8];
#pragma unroll
  for (int i = 0; i < 8; ++i) og[i] = BLD(WS_O + 2097152 + ofo, i * 1024);
  SBAR();
  RDWO(1, 0); RDWO(1, 1); RDWO(1, 2); RDWO(1, 3);
  LBAR();
  PUTP(L_SCR);
  LBAR();
  SUMP(L_SCR, 0);
  ST(7);
#define CHECKP(i) do { _Pragma("unroll 1") for (u32 spins = 0; spins < SPIN_CAP; ++spins) { \
      const u32 mx_ = max(max(max(og[2 * (i)][0], og[2 * (i)][1]), max(og[2 * (i)][2], og[2 * (i)][3])), max(max(og[2 * (i) + 1][0], og[2 * (i) + 1][1]), max(og[2 * (i) + 1][2], og[2 * (i) + 1][3]))); \
      if (__builtin_expect(__all(mx_ != SENT), 1)) break; \
      __builtin_amdgcn_s_sleep(2); \
      og[2 * (i)] = BLD(WS_O + 2097152 + ofo, (2 * (i)) * 1024); og[2 * (i) + 1] = BLD(WS_O + 2097152 + ofo, (2 * (i) + 1) * 1024); } } while (0)
  CHECKP(0); MMO(og, 0);
  CHECKP(1); MMO(og, 1);
  CHECKP(2); MMO(og, 2);
  CHECKP(3);
  ST(8);
  MMO(og, 3);
  PUTP(0);
  LBAR();
  SUMP(0, 1);
  ST(9);
  asm volatile(".Lfa_end:");
}

extern "C" void kernel_launch(void* const* d_in, const int* in_sizes, int n_in, void* d_out, int out_size, void* d_ws, size_t ws_size,
                              hipStream_t stream) {
  fused_attn<<<256, 512, 0, stream>>>((const char*)d_in[0], (const char*)d_in[1], (const char*)d_in[2], (const char*)d_in[3],
                                      (const char*)d_in[4], (const char*)d_in[5], (char*)d_out, (char*)d_ws);
}
```

```cpp
#define ECHO_A 0
#define ECHO_B 0
#include <hip/hip_runtime.h>
#include <hip/hip_bf16.h>

typedef unsigned u32;
typedef unsigned long long u64;
typedef short bf16x8 __attribute__((ext_vector_type(8)));
typedef float f32x16 __attribute__((ext_vector_type(16)));
typedef float f32x4 __attribute__((ext_vector_type(4)));
typedef float f32x2 __attribute__((ext_vector_type(2)));
typedef u32 u32x4 __attribute__((ext_vector_type(4)));
typedef u32 u32x2 __attribute__((ext_vector_type(2)));
typedef __bf16 bf2_t __attribute__((ext_vector_type(2)));
#define LAS __attribute__((address_space(3)))
#define GAS __attribute__((address_space(1)))
#define DI __device__ __forceinline__
#define SBAR() __builtin_amdgcn_sched_barrier(0)
#define PIN(x) asm volatile("" : "+v"(x))
#define MFMA(a, b, c) __builtin_amdgcn_mfma_f32_32x32x16_bf16(__builtin_bit_cast(bf16x8, a), __builtin_bit_cast(bf16x8, b), (c), 0, 0, 0)
#define RLX_AGENT __ATOMIC_RELAXED, __HIP_MEMORY_SCOPE_AGENT
#define BLD(voff, soff) __builtin_amdgcn_raw_buffer_load_b128(rs, (voff), (soff), 16)
#define BST(v, voff) __builtin_amdgcn_raw_buffer_store_b128((v), rs, (voff), 0, 16)

#define POLL_SLEEP 4
#define G1PRIO 3
#define ROT 7
#define XPF_STEP 1
#define PFLAG_STEP 1

constexpr u32 WS_GEN = 0;
constexpr u32 WS_FST = 2048;
constexpr u32 WS_FT = 4096;
constexpr u32 WS_FO = WS_FT + 8192;
constexpr u32 WS_FS = WS_FO + 8192;
constexpr u32 WS_FP = WS_FS + 8192;
constexpr u32 WS_T = 65536;
constexpr u32 WS_O = WS_T + 6291456;
constexpr u32 WS_P = WS_O + 2 * 2097152;
constexpr u32 WS_END = WS_P + 16 * 86016;
constexpr u32 WS_STAMP = 16u << 20;

constexpr u32 SLOT_B = 30720, SLOT_HALO = 16384, SLOT_W = 18432;
constexpr u32 L_Q = 4 * SLOT_B;
constexpr u32 L_X = L_Q + 8192;
constexpr u32 L_WO = 0, WO_B = 50176;
constexpr u32 L_SCR = 2 * WO_B;
constexpr u32 L_LP = L_SCR + 57344;
constexpr u32 L_SYNC = L_LP + 4096;
constexpr u32 LDS_BYTES = 163840;

constexpr float SCALE = 0.18898223650461362f;
constexpr float LOG2E = 1.4426950408889634f;
constexpr float QSC = SCALE * LOG2E;
constexpr float MSHIFT = 40.0f;
constexpr u32 SPIN_CAP = 1u << 17;
constexpr u32 SENT = 0xFFFFFFFFu;

DI u32 pk(float lo, float hi) { f32x2 v = {lo, hi}; return __builtin_bit_cast(u32, __builtin_convertvector(v, bf2_t)); }
DI u32x4 pk8(const f32x16& a, int b) { u32x4 r = {pk(a[b], a[b + 1]), pk(a[b + 2], a[b + 3]), pk(a[b + 4], a[b + 5]), pk(a[b + 6], a[b + 7])}; return r; }
DI float blo(u32 x) { return __uint_as_float(x << 16); }
DI float bhi(u32 x) { return __uint_as_float(x & 0xffff0000u); }
DI float sel(bool c, float a, float b) { PIN(a); PIN(b); return c ? a : b; }
DI float swapmax(float m) { auto r = __builtin_amdgcn_permlane32_swap(__float_as_uint(m), __float_as_uint(m), false, false); return fmaxf(__uint_as_float(r[0]), __uint_as_float(r[1])); }
DI float swapadd(float m) { auto r = __builtin_amdgcn_permlane32_swap(__float_as_uint(m), __float_as_uint(m), false, false); return __uint_as_float(r[0]) + __uint_as_float(r[1]); }
#define NTS ""
DI void glds16(u32 voff, const void* sbase, u32 ldsaddr) {
  u32 keep;
  asm volatile("s_mov_b32 %0, m0\n\ts_mov_b32 m0, %3\n\ts_nop 0\n\tglobal_load_lds_dwordx4 %1, %2" NTS "\n\ts_mov_b32 m0, %0"
               : "=&s"(keep) : "v"(voff), "s"(sbase), "s"(ldsaddr) : "memory");
}
DI void glds16_sc1(u32 voff, const void* sbase, u32 ldsaddr) {
  u32 keep;
  asm volatile("s_mov_b32 %0, m0\n\ts_mov_b32 m0, %3\n\ts_nop 0\n\tglobal_load_lds_dwordx4 %1, %2 sc1\n\ts_mov_b32 m0, %0"
               : "=&s"(keep) : "v"(voff), "s"(sbase), "s"(ldsaddr) : "memory");
}
DI void touch_l2(u32 voff, const void* sbase, u32 ldsaddr) {
  u32 keep;
  asm volatile("s_mov_b32 %0, m0\n\ts_mov_b32 m0, %3\n\ts_nop 0\n\tglobal_load_lds_dword %1, %2" NTS "\n\ts_mov_b32 m0, %0"
               : "=&s"(keep) : "v"(voff), "s"(sbase), "s"(ldsaddr) : "memory");
}
DI void glds63(u32 voff, const void* sbase, u32 ldsaddr) {
  u32 keep;
  asm volatile("s_mov_b32 %0, m0\n\ts_mov_b32 m0, %3\n\ts_bitset0_b32 exec_hi, 31\n\tglobal_load_lds_dwordx4 %1, %2\n\ts_bitset1_b32 exec_hi, 31\n\ts_mov_b32 m0, %0"
               : "=&s"(keep) : "v"(voff), "s"(sbase), "s"(ldsaddr) : "memory");
}
DI u32x4 ldsr128(const LAS char* p) { return *(const LAS u32x4*)p; }
DI void ldsw128(LAS char* p, u32x4 v) { *(LAS u32x4*)p = v; }
typedef short v4i16_t __attribute__((ext_vector_type(4)));
DI u32x2 ldstr(const LAS char* p) { return __builtin_bit_cast(u32x2, __builtin_amdgcn_ds_read_tr16_b64_v4i16((LAS v4i16_t*)p)); }
DI u32x4 trfrag(const LAS char* p0, const LAS char* p1) { u32x2 a = ldstr(p0), b = ldstr(p1); u32x4 r = {a[0], a[1], b[0], b[1]}; return r; }
#define LBAR() do { __builtin_amdgcn_s_waitcnt(0xC07F); asm volatile("s_barrier" ::: "memory"); SBAR(); } while (0)

DI void lds_set(LAS char* p) { asm volatile("" ::: "memory"); __hip_atomic_store((LAS u32*)p, 1u, __ATOMIC_RELAXED, __HIP_MEMORY_SCOPE_WORKGROUP); asm volatile("" ::: "memory"); }
DI void lds_wait(const LAS char* p, u32 want) {
#pragma unroll 1
  for (u32 spins = 0; spins < (SPIN_CAP << 4); ++spins) {
    const u32 v = __hip_atomic_load((const LAS u32*)p, __ATOMIC_RELAXED, __HIP_MEMORY_SCOPE_WORKGROUP);
    if (__all(v == want)) break;
    __builtin_amdgcn_s_sleep(1);
  }
  asm volatile("" ::: "memory");
}
DI void wait_flags(const char* ws, u32 laneoff, u64 tag) {
  const GAS u64* f = (const GAS u64*)(ws + laneoff);
#pragma unroll 1
  for (u32 spins = 0; spins < SPIN_CAP; ++spins) {
    u64 v = __hip_atomic_load(f, RLX_AGENT);
    if (__all(v == tag)) break;
    __builtin_amdgcn_s_sleep(POLL_SLEEP);
  }
  __builtin_amdgcn_fence(__ATOMIC_ACQUIRE, "wavefront");
  asm volatile("" ::: "memory");
}
DI void set_flag(char* ws, u32 off, u64 tag) { __hip_atomic_store((GAS u64*)(ws + off), tag, RLX_AGENT); }

#define ST(i) do { if ((i) == ECHO_A) echo_a = __builtin_amdgcn_s_memrealtime(); if ((i) == ECHO_B) echo_b = __builtin_amdgcn_s_memrealtime(); \
    if ((i) == 9 && w == 0) { const u64 e_ = __builtin_amdgcn_s_memrealtime() + (echo_b - echo_a); while (__builtin_amdgcn_s_memrealtime() < e_) __builtin_amdgcn_s_sleep(1); } } while (0)

#define TP(i) do {} while (0)
#define LWAIT(N, c) asm volatile("s_waitcnt vmcnt(" #N ")\n\ts_barrier" ::: "memory")
DI void lds_add1(LAS char* p, int lane) { asm volatile("" ::: "memory"); if (lane == 0) __hip_atomic_fetch_add((LAS u32*)p, 1u, __ATOMIC_RELAXED, __HIP_MEMORY_SCOPE_WORKGROUP); asm volatile("" ::: "memory"); }
#define SLOTADDR(c) do { const u32 sb_ = (u32)((c) & 3) * SLOT_B; xa = sb_ + xa0; xh = sb_ + xh0; wp = lds + sb_ + wa0; } while (0)
#define RDX(j) x[j] = ldsr128(lds + (xa ^ ((j) << 5)))
#define RDH(j) do { if (HALO) hx[j] = ldsr128(lds + (xh ^ ((j) << 5))); } while (0)
#define RDW(t, j) f[t][j] = PACKED ? ldsr128(wp + (j) * 3072 + (t) * 1024) : trfrag(wp + (j) * 3072 + (t) * 56, wp + (j) * 3072 + 768 + (t) * 56)
#define MM(A, t, j) A = ((t) >= 3 - NV) ? MFMA(x[j], f[t][j], A) : MFMA(f[t][j], x[j], A)
#define MH(j) do { if (HALO == 1) ah = MFMA(f[1][j], hx[j], ah); if (HALO == 2) ah = MFMA(hx[j], f[2][j], ah); } while (0)
#define HALF(m, r, I0, I1, I2, I3) do { \
    MM(a0, 0, m); SBAR(); RDX(r); RDW(0, r); SBAR(); \
    MM(a1, 1, m); SBAR(); RDW(1, r); I0; SBAR(); \
    MM(a2, 2, m); SBAR(); RDW(2, r); RDH(r); I1; SBAR(); \
    MH(m); SBAR(); \
    MM(a0, 0, m + 1); SBAR(); RDX(r + 1); RDW(0, r + 1); I2; SBAR(); \
    MM(a1, 1, m + 1); SBAR(); RDW(1, r + 1); RDW(2, r + 1); RDH(r + 1); SBAR(); \
    MM(a2, 2, m + 1); SBAR(); I3; SBAR(); \
    MH(m + 1); SBAR(); } while (0)
#define RDFIRST() do { RDX(0); RDW(0, 0); RDW(1, 0); RDW(2, 0); RDH(0); RDX(1); RDW(0, 1); RDW(1, 1); RDW(2, 1); RDH(1); SBAR(); } while (0)

template <int NV>
DI void gemmB(LAS char* lds, u32 xa0, u32 wa0, f32x16& a0, f32x16& a1, f32x16& a2, const char* ws, int fstep, u32 flagoff, u64 tag, int lane) {
  constexpr bool PACKED = false; constexpr int HALO = 0;
  u32x4 x[4], f[3][4], hx[4]; f32x16 ah;
  u32 xa, xh; const u32 xh0 = 0; const LAS char* wp;
  LBAR();
  SLOTADDR(0);
  RDFIRST();
#pragma unroll 1
  for (int c = 0; c < 7; ++c) {
    HALF(0, 2, , , , );
    asm volatile("s_cmp_eq_u32 %0, %1\n\ts_cbranch_scc0 1f\n\ts_waitcnt vmcnt(0)\n\ts_mov_b64 exec, 3\n\tglobal_store_dwordx2 %2, %3, %4 sc1\n\ts_mov_b64 exec, -1\n1:"
                 :: "s"(c), "s"(fstep), "v"(flagoff), "v"(tag), "s"(ws) : "memory", "scc");
    __builtin_amdgcn_s_waitcnt(0xC07F);
    asm volatile("s_cmp_eq_u32 %0, 6\n\ts_cbranch_scc1 1f\n\ts_barrier\n1:" :: "s"(c) : "memory", "scc");
    SBAR();
    SLOTADDR(c + 1);
    HALF(2, 0, , , , );
  }
}

template <int HALO>
DI void gemmA(LAS char* lds, u32 xa0, u32 xh0, u32 wa0, f32x16& a0, f32x16& a1, f32x16& a2, f32x16& ah) {
  constexpr bool PACKED = true; constexpr int NV = 1;
  u32x4 x[4], f[3][4], hx[4];
  u32 xa, xh; const LAS char* wp;
  SLOTADDR(7);
  RDFIRST();
#pragma unroll 1
  for (int c = 7; c < 14; ++c) {
    HALF(0, 2, , , , );
    __builtin_amdgcn_s_waitcnt(0xC07F);
    asm volatile("s_waitcnt vmcnt(0)\n\ts_cmp_eq_u32 %0, 13\n\ts_cbranch_scc1 1f\n\ts_barrier\n1:" :: "s"(c) : "memory", "scc");
    SBAR();
    SLOTADDR(c + 1);
    HALF(2, 0, , , , );
  }
}

__global__ __launch_bounds__(512, 2) void fused_attn(const char* __restrict__ Ax, const char* __restrict__ Aw, const char* __restrict__ Ao,
                                                     const char* __restrict__ Bx, const char* __restrict__ Bw, const char* __restrict__ Bo,
                                                     char* __restrict__ out, char* ws) {
  __shared__ __attribute__((aligned(16))) char lds_raw[LDS_BYTES];
  LAS char* lds = (LAS char*)lds_raw;
  const u32 lds0 = (u32)(size_t)lds;
  const int tid = threadIdx.x, lane = tid & 63, r32 = lane & 31, hh = lane >> 5;
  const int w = __builtin_amdgcn_readfirstlane(tid >> 6);
  const int bid = blockIdx.x, xcd = bid & 7, slot = bid >> 3;
  const int rb = 4 * (xcd & 3) + (slot & 3), g = 8 * (xcd >> 2) + (slot >> 2);
  const int h = 2 * xcd + (slot >> 4), qb = slot & 15;
  const int cg = slot & 7, ot0 = 8 * xcd + 2 * (slot >> 3);
  const int tq = w & 3;
  asm volatile("" :: "s"(Ax), "s"(Aw), "s"(Ao), "s"(Bx), "s"(Bw), "s"(Bo), "s"(out), "s"(ws));
  u64 echo_a = 0, echo_b = 0;
  ST(0);
  u64 stv = 0;
  const bool packer = w < 3 && rb < 14;
  if (packer) {
    const u32 vo = (u32)(32 * rb + (lane >> 2)) * 2688 + tq * 896 + 56 * g - 8 * (g & 1) + (lane & 3) * 16;
    glds16(vo, Aw, lds0 + L_X + tq * 2560); glds16(vo, Aw + 16 * 2688, lds0 + L_X + tq * 2560 + 1024);
  }
  int kc = (g & 7) + 2 * (rb & 3); kc = kc >= 7 ? kc - 7 : kc;
  const int p0 = (tq & 1) + 8 * (tq >> 1);
  const u32 voffX = (u32)(128 * rb + 8 * p0 + (lane >> 3)) * 896u + (u32)(((lane & 7) ^ ((4 * (tq & 1) + (lane >> 4)) & 7)) << 4);
  { const int j0 = (w >> 2) * 2;
    glds16(voffX, Bx + kc * 128 + j0 * 14336, lds0 + (p0 + 2 * j0) * 1024);
    glds16(voffX, Bx + kc * 128 + (j0 + 1) * 14336, lds0 + (p0 + 2 * j0 + 2) * 1024); }
  u32 voffW[3];
#pragma unroll
  for (int j = 0; j < 3; ++j) { const int i = 64 * j + lane, kr = (i * 171) >> 11, rem = i - kr * 12; voffW[j] = (16 * tq + kr) * 2688 + 168 * g - 8 * (g & 1) + min(rem, 10) * 16; }
  if (w >= 4) { glds16(voffW[0], Bw + kc * (64 * 2688), lds0 + SLOT_W + (3 * tq) * 1024); glds16(voffW[1], Bw + kc * (64 * 2688), lds0 + SLOT_W + (3 * tq + 1) * 1024); }
  else glds16(voffW[2], Bw + kc * (64 * 2688), lds0 + SLOT_W + (3 * tq + 2) * 1024);
  kc = kc == 6 ? 0 : kc + 1;

  if (w == 3) {
    u64 pc; u32 len;
    asm volatile("s_getpc_b64 %0\n.Lfa_pc:\n\ts_mov_b32 %1, .Lfa_end-.Lfa_pc" : "=s"(pc), "=s"(len));
    touch_l2(min((u32)(slot + 32 * (lane & 15)) * 128u, len), (const void*)pc, lds0 + L_SYNC + 1024);
  }
  const __amdgpu_buffer_rsrc_t rs = __builtin_amdgcn_make_buffer_rsrc(ws, 0, WS_END, 0x00020000);
  const f32x16 zero16 = {};
  u64 tag;
  u32x4 qf[4][2];
  u32x4 kv[8][4];
  const u32 kvb = WS_T + (u32)(((16 + h) * 64 + 8 * w) * 2) * 1024 + lane * 16;
  const int trot = qb >> 1;
  const int ptile = 4 * rb + tq;
#define LOADF(t, f) kv[t][f] = BLD(kvb + ((f) & 1) * 1024, (((t) + trot) & 7) * 2048 + ((f) >> 1) * 2097152)
#define LOADT(t) do { LOADF(t, 0); LOADF(t, 1); LOADF(t, 2); LOADF(t, 3); } while (0)
#define READQ() do { _Pragma("unroll") for (int i_ = 0; i_ < 4; ++i_) { const u32 a_ = L_Q + (u32)((i_ ^ (w >> 1)) * 2048) + lane * 16; \
      qf[i_][0] = ldsr128(lds + a_); qf[i_][1] = ldsr128(lds + a_ + 1024); } } while (0)
#define GETTAG() do { const u64 gen_ = *(const __attribute__((address_space(4))) u64*)(ws + WS_GEN); \
      tag = gen_ * 6364136223846793005ull + 1442695040888963407ull; asm volatile("" : "+s"(tag)); } while (0)
  u32 voffH;
  { const int hr = 8 * (tq & 1) + (lane >> 3); int tok = 128 * rb - 16 + hr; tok = tok < 0 ? 0 : tok;
    voffH = (u32)tok * 896u + (u32)(((lane & 7) ^ ((hr >> 1) & 7)) << 4); }
  auto issueA = [&](int cc) {
    const u32 sb = lds0 + (u32)(cc & 3) * SLOT_B;
    const char* xs = Ax + kc * 128; const char* wq = ws + WS_P + g * 86016 + kc * 12288 + tq * 3072;
    kc = kc == 6 ? 0 : kc + 1;
#pragma unroll
    for (int j = 0; j < 4; ++j) glds16(voffX, xs + j * 14336, sb + (p0 + 2 * j) * 1024);
    if (tq < 2) glds16(voffH, xs, sb + SLOT_HALO + tq * 1024);
#pragma unroll
    for (int j = 0; j < 3; ++j) glds16_sc1((u32)lane * 16, wq + j * 1024, sb + SLOT_W + (3 * tq + j) * 1024);
  };
  const int xrow = 32 * tq + r32;
  const u32 xa0 = xrow * 128 + ((hh ^ ((xrow >> 1) & 7)) << 4);
  const u32 xh0 = SLOT_HALO + (r32 & 15) * 128 + ((hh ^ (((r32 & 15) >> 1) & 7)) << 4);
  f32x16 aq = zero16, ak = zero16, av = zero16, ah = zero16;

  if (w >= 4) {
    auto issueB = [&](int cc) {
      const u32 sb = lds0 + (u32)(cc & 3) * SLOT_B;
      const char* xs = Bx + kc * 128; const char* wq = Bw + kc * (64 * 2688);
      kc = kc == 6 ? 0 : kc + 1;
#pragma unroll
      for (int j = 0; j < 4; ++j) {
        glds16(voffX, xs + j * 14336, sb + (p0 + 2 * j) * 1024);
        if (j < 3) glds16(voffW[j], wq, sb + SLOT_W + (3 * tq + j) * 1024);
      }
    };
#define XTOUCH() do { int row0 = 128 * rb - 16 + 18 * (g & 7); row0 = row0 < 0 ? 0 : row0; \
      touch_l2((u32)row0 * 896u + (u32)min(tq * 32 + r32, 125) * 128u, Ax, lds0 + L_SYNC + 1024); } while (0)
    issueB(1);
    if (XPF_STEP < 0) XTOUCH();
    GETTAG();
    const u32 pflag = WS_FP + (u32)(g * 64 + (lane < 42 ? lane : 0)) * 8;
    u64 pfv = 0;
#pragma unroll 1
    for (int c = 0; c < 5; ++c) {
      { u32 t_; asm volatile("s_sub_u32 %0, %1, %2\n\ts_cmp_lt_u32 %0, 2\n\ts_cbranch_scc1 1f\n\ts_waitcnt vmcnt(7)\n1:" : "=&s"(t_) : "s"(c), "n"(XPF_STEP + 1) : "memory", "scc"); }
      LWAIT(8, c);
      asm volatile("s_cmp_eq_u32 %3, %4\n\ts_cbranch_scc0 1f\n\tglobal_load_dwordx2 %0, %1, %2 sc1\n1:" : "+v"(pfv) : "v"(pflag), "s"(ws), "s"(c), "s"(w) : "memory", "scc");
      issueB(c + 2);
      if (c == XPF_STEP) XTOUCH();
    }
    asm volatile("s_waitcnt vmcnt(7)" : "+v"(pfv) :: "memory");
    if (w == 4 && !__all(pfv == tag)) wait_flags(ws, pflag, tag);
    LWAIT(7, 5);
    issueA(7);
    LWAIT(7, 6);
    issueA(8);
    ST(1);
    asm volatile("s_waitcnt vmcnt(7)\n\ts_barrier" ::: "memory");
    { const u32 wp0 = SLOT_W + lane * 16;
      if (tq == 2) gemmA<1>(lds, xa0, xh0, wp0, aq, ak, av, ah);
      else if (tq == 3) gemmA<2>(lds, xa0, xh0, wp0, aq, ak, av, ah);
      else gemmA<0>(lds, xa0, xh0, wp0, aq, ak, av, ah); }
    ST(2);
    TP(60);
    __builtin_amdgcn_s_setprio(3);
    LAS char* xb = lds + L_X;
    u32x4 aq0 = pk8(aq, 0), aq1 = pk8(aq, 8), ak0 = pk8(ak, 0), ak1 = pk8(ak, 8), cv0 = pk8(av, 0), cv1 = pk8(av, 8);
    if (hh) { aq1[2] = 0; aq1[3] = 0; ak1[2] = 0; ak1[3] = 0; }
    if (r32 >= 28) { cv0 = (u32x4){0, 0, 0, 0}; cv1 = cv0; }
    if (tq < 3) { LAS char* p = xb + (tq + 1) * 3072 + lane * 16; ldsw128(p, ak0); ldsw128(p + 1024, ak1); ldsw128(p + 2048, cv1); }
    if (tq == 2) {
      u32x4 h0 = pk8(ah, 0), h1 = pk8(ah, 8);
      if (hh) { h1[2] = 0; h1[3] = 0; }
      ldsw128(xb + lane * 16, h0); ldsw128(xb + 1024 + lane * 16, h1);
    } else if (tq == 3) {
      u32x4 h1 = pk8(ah, 8);
      if (r32 >= 28) h1 = (u32x4){0, 0, 0, 0};
      ldsw128(xb + 2048 + lane * 16, h1);
    }
    lds_set(lds + L_SYNC + 16 + tq * 4);
    f32x16 sc = MFMA(ak0, aq0, zero16); sc = MFMA(ak1, aq1, sc);
    lds_wait(lds + L_SYNC + 16 + (lane & 3) * 4, 1);
    u32x4 pk0 = ldsr128(xb + tq * 3072 + lane * 16), pk1 = ldsr128(xb + tq * 3072 + 1024 + lane * 16), pv1 = ldsr128(xb + tq * 3072 + 2048 + lane * 16);
    f32x16 sp = MFMA(pk0, aq0, zero16); sp = MFMA(pk1, aq1, sp);
    const bool odd = r32 & 1; const bool pvalid = ptile != 0;
    float t[12];
    float mx = -INFINITY;
#pragma unroll
    for (int i = 0; i < 12; ++i) {
      const int rp = i < 8 ? 2 * i : 2 * i - 8;
      const float sv = i < 8 ? sel(odd, sc[rp + 1], sc[rp]) : sel(odd, sp[rp + 1], sp[rp]);
      const int rho = (rp & 3) + 8 * (rp >> 2) + 4 * hh;
      const int dl = (r32 & ~1) - rho + (i < 8 ? 0 : 32);
      const bool ok = (unsigned)dl <= 14u && (i < 8 || pvalid);
      const float l1 = blo(pk(sv, 0.f)) * SCALE;
      const float l2 = blo(pk(l1, 0.f));
      t[i] = ok ? l2 : -INFINITY;
      mx = fmaxf(mx, t[i]);
    }
    mx = swapmax(mx);
    float sum = 0.f;
#pragma unroll
    for (int i = 0; i < 12; ++i) { t[i] = __builtin_amdgcn_exp2f((t[i] - mx) * LOG2E); sum += t[i]; }
    sum = swapadd(sum);
    const float inv = __builtin_amdgcn_rcpf(sum);
    u32 pw[12];
#pragma unroll
    for (int i = 0; i < 12; ++i) { const u32 b = pk(t[i] * inv, 0.f); pw[i] = odd ? (b << 16) : b; }
    const u32x4 p0 = {pw[0], pw[1], pw[2], pw[3]}, p1 = {pw[4], pw[5], pw[6], pw[7]}, pp = {pw[8], pw[9], pw[10], pw[11]};
    f32x16 oa = MFMA(cv0, p0, zero16); oa = MFMA(cv1, p1, oa); oa = MFMA(pv1, pp, oa);
    BST(pk8(oa, 0), WS_O + (u32)(ptile * 16 + g) * 2048 + lane * 16);
    BST(pk8(oa, 8), WS_O + (u32)(ptile * 16 + g) * 2048 + 1024 + lane * 16);
    lds_wait(lds + L_SYNC + (lane & 3) * 4, 1);
    LOADT(0); LOADT(1);
    READQ();
  } else {
    if (tid < 16) *(LAS u32*)(lds + L_SYNC + tid * 4) = 0;
    GETTAG();
    asm volatile("s_waitcnt vmcnt(3)" ::: "memory");
    if (packer) {
      const LAS char* pp = lds + L_X + tq * 2560 + (8 * hh + ((lane & 15) >> 2)) * 64 + 8 * (g & 1) + ((lane >> 4) & 1) * 32 + (lane & 3) * 8;
      const u32x4 f0 = trfrag(pp, pp + 256), f1 = trfrag(pp + 1024, pp + 1280);
      const u32 po = WS_P + (u32)(g * 84 + 6 * rb + tq) * 1024 + lane * 16;
      BST(f0, po); BST(f1, po + 3072);
      asm volatile("s_waitcnt vmcnt(2)" ::: "memory");
    } else asm volatile("s_waitcnt vmcnt(0)" ::: "memory");
    { const u32 wa0 = SLOT_W + (8 * hh + ((lane & 15) >> 2)) * 192 + 8 * (g & 1) + ((lane >> 4) & 1) * 32 + (lane & 3) * 8;
      const int fs = (packer || w == 0) ? PFLAG_STEP : 99;
      const u32 fo = (packer && !(w == 0 && lane == 1)) ? WS_FP + (u32)(g * 64 + 3 * rb + tq) * 8 : WS_FST + (u32)bid * 8;
      if (g < 10) gemmB<0>(lds, xa0, wa0, aq, ak, av, ws, fs, fo, tag, lane);
      else if (g == 10) gemmB<1>(lds, xa0, wa0, aq, ak, av, ws, fs, fo, tag, lane);
      else gemmB<3>(lds, xa0, wa0, aq, ak, av, ws, fs, fo, tag, lane); }
    ST(1);
    __builtin_amdgcn_s_setprio(3);
    const u32 to = WS_T + (u32)((3 * g * 64 + ptile) * 2) * 1024 + lane * 16;
    auto put = [&](f32x16& a, int j) {
      const int sl = 3 * g + j;
      if (sl < 16) a *= QSC;
      u32x4 t0 = pk8(a, 0), t1 = pk8(a, 8);
      if (sl < 32) { if (hh) { t1[2] = sl < 16 ? 0u : 0x00003F80u; t1[3] = 0; } }
      else if (r32 >= 28) { const u32 f = r32 == 28 ? 0x3F803F80u : 0u; t0 = (u32x4){f, f, f, f}; t1 = t0; }
      BST(t0, to + j * 131072); BST(t1, to + j * 131072 + 1024);
    };
    kc = kc == 6 ? 0 : kc + 1;
    __builtin_amdgcn_s_waitcnt(0xC07F);
    LWAIT(63, 7);
    put(aq, 0); put(ak, 1); put(av, 2);
    LWAIT(63, 8);
    issueA(9);
    asm volatile("s_waitcnt vmcnt(7)" ::: "memory");
    u32 prevs;
    { u32 prev = 0;
      if (lane == 0) prev = __hip_atomic_fetch_add((LAS u32*)(lds + L_SYNC + 40), 1u, __ATOMIC_RELAXED, __HIP_MEMORY_SCOPE_WORKGROUP);
      prevs = __builtin_amdgcn_readfirstlane(prev);
      if (prevs == 3 && lane < 3) set_flag(ws, WS_FT + (u32)((3 * g + lane) * 16 + rb) * 8, tag); }
    issueA(10);
    const u32 tflag = WS_FT + (u32)((lane & 32) ? h * 16 + qb : (16 + (lane & 16) + h) * 16 + (lane & 15)) * 8;
    u64 tfv = 0;
    asm volatile("s_cmp_eq_u32 %0, 3\n\ts_cbranch_scc1 1f\n\ts_waitcnt vmcnt(7)\n1:" :: "s"(prevs) : "memory", "scc");
    LWAIT(8, 9);  issueA(11);
    LWAIT(7, 10); issueA(12);
    LWAIT(7, 11); issueA(13);
    LWAIT(7, 12);
    asm volatile("s_cmp_eq_u32 %3, 0\n\ts_cbranch_scc0 1f\n\tglobal_load_dwordx2 %0, %1, %2 sc1\n\ts_waitcnt vmcnt(1)\n\ts_branch 2f\n1:\n\ts_waitcnt vmcnt(0)\n2:"
                 : "+v"(tfv) : "v"(tflag), "s"(ws), "s"(w) : "memory", "scc");
    LWAIT(1, 13);
    TP(28);
    if (w == 0) {
      asm volatile("s_waitcnt vmcnt(0)" : "+v"(tfv) :: "memory");
      if (!__all(tfv == tag)) wait_flags(ws, tflag, tag);
      lds_set(lds + L_SYNC + 44);
    } else lds_wait(lds + L_SYNC + 44, 1);
    TP(29);
    u32x4 q0 = BLD(WS_T + (u32)((h * 64 + 4 * qb + tq) * 2) * 1024 + lane * 16, 0), q1 = BLD(WS_T + (u32)((h * 64 + 4 * qb + tq) * 2) * 1024 + lane * 16, 1024);
    LOADT(0); LOADT(1);
    SBAR();
    f32x16 s = MFMA(kv[0][0], q0, zero16); s = MFMA(kv[0][1], q1, s);
    ldsw128(lds + L_Q + tq * 2048 + lane * 16, q0);
    float m = s[0];
#pragma unroll
    for (int i = 1; i < 16; ++i) m = fmaxf(m, s[i]);
    m = swapmax(m);
    if (hh) q1[2] = pk(-(m + MSHIFT), 0.f);
    ldsw128(lds + L_Q + tq * 2048 + 1024 + lane * 16, q1);
    lds_set(lds + L_SYNC + tq * 4);
    ST(2);
    TP(30);
    lds_wait(lds + L_SYNC + (lane & 3) * 4, 1);
    READQ();
    TP(31);
  }
  __builtin_amdgcn_s_waitcnt(0xC07F);
  if (lane == 0) __hip_atomic_fetch_add((LAS u32*)(lds + L_SYNC + 32), 1u, __ATOMIC_RELAXED, __HIP_MEMORY_SCOPE_WORKGROUP);
  ST(3);
  const int qt2 = w & 1, kq = w >> 1, otile = ot0 + qt2;
  const u32 ofo = (u32)(otile * 16 + 4 * kq) * 2048 + lane * 16;
  u32x4 of[8];
  f32x16 o[4];
  {
    const u32 voffO = (u32)((lane / 7) * 896 + (lane % 7) * 16 + cg * 112);
    const int wr6 = w < 2 ? (w == 0 ? 432 : 439) : 9 * w + 360;
#define WOP(j) do { const int jj_ = (j) % 7, r_ = jj_ < 6 ? 9 * w + 72 * jj_ : wr6; \
      glds63(voffO, ((j) >= 7 ? Bo : Ao) + r_ * 896, lds0 + ((j) >= 7 ? WO_B : 0) + r_ * 112); } while (0)
    const u32 fao = WS_FO + (u32)(lane & 4) * 2048 + (u32)(otile * 16 + 4 * kq + (lane & 3)) * 8;
    const GAS u64* fa = (const GAS u64*)(ws + fao);
    u64 fav;
    f32x16 s[2]; u32x4 pa, pb;
    __builtin_amdgcn_s_setprio(2);
    SBAR();
    s[0] = MFMA(kv[0][0], qf[0][0], zero16); s[0] = MFMA(kv[0][1], qf[0][1], s[0]);
    SBAR();
#define EXP(x) x = __builtin_amdgcn_exp2f(x)
#define STEP(u) do { \
      constexpr int kt = (u) >> 2, qt = (u) & 3, un = (u) + 1, ktn = (un >> 2) & 7, qtn = un & 3, up = (u) > 0 ? (u) - 1 : 0, kl = (kt + 3) & 7; \
      f32x16& sc = s[(u) & 1]; f32x16& sn = s[un & 1]; \
      if (un < 32) { sn = MFMA(kv[ktn][0], qf[qtn][0], zero16); PIN(sn); } SBAR(); \
      if ((u) < 4) LOADF(2, qt); \
      if (kt + 3 < 8) LOADF(kl, qt); \
      if ((u) >= 24) of[(u) & 7] = BLD(WS_O + ofo, ((u) & 7) * 1024); \
      EXP(sc[0]); EXP(sc[1]); EXP(sc[2]); EXP(sc[3]); PIN(sc); SBAR(); \
      if ((u) > 0) { o[up & 3] = MFMA(kv[up >> 2][3], pb, o[up & 3]); PIN(o[up & 3]); } SBAR(); \
      EXP(sc[4]); EXP(sc[5]); PIN(sc); SBAR(); pa[0] = pk(sc[0], sc[1]); pa[1] = pk(sc[2], sc[3]); PIN(pa); SBAR(); EXP(sc[6]); EXP(sc[7]); PIN(sc); SBAR(); \
      if (un < 32) { sn = MFMA(kv[ktn][1], qf[qtn][1], sn); PIN(sn); } SBAR(); \
      if ((u) >= 12 && (u) < 26) WOP((u) - 12); \
      EXP(sc[8]); PIN(sc); SBAR(); pa[2] = pk(sc[4], sc[5]); pa[3] = pk(sc[6], sc[7]); PIN(pa); SBAR(); EXP(sc[9]); EXP(sc[10]); EXP(sc[11]); PIN(sc); SBAR(); \
      o[qt] = MFMA(kv[kt][2], pa, (u) < 4 ? zero16 : o[qt]); PIN(o[qt]); SBAR(); \
      EXP(sc[12]); EXP(sc[13]); EXP(sc[14]); EXP(sc[15]); PIN(sc); SBAR(); \
      pb[0] = pk(sc[8], sc[9]); pb[1] = pk(sc[10], sc[11]); pb[2] = pk(sc[12], sc[13]); pb[3] = pk(sc[14], sc[15]); PIN(pb); SBAR(); \
      if ((u) == 15) __builtin_amdgcn_s_setprio(1); \
      if ((u) == 26) __builtin_amdgcn_s_setprio(0); \
    } while (0)
#define STEP4(t) STEP(4 * (t)); STEP(4 * (t) + 1); STEP(4 * (t) + 2); STEP(4 * (t) + 3)
    STEP4(0);
    touch_l2((u32)(slot * 98 + min((w & 3) * 32 + r32, 97)) * 128u, w < 4 ? Ao : Bo, lds0 + L_SYNC + 1024);
    {
      const u32x4 sv = {SENT, SENT, SENT, SENT};
      asm volatile("s_cmp_lt_u32 %0, 4\n\ts_cbranch_scc0 1f\n\tbuffer_store_dwordx4 %1, %2, %3, 0 offen sc1\n\tbuffer_store_dwordx4 %1, %2, %3, 0 offen offset:1024 sc1\n\ts_nop 1\n1:"
                   :: "s"(w), "v"(sv), "v"(WS_O + 2097152 + (u32)((4 * qb + tq) * 16 + h) * 2048 + lane * 16), "s"(rs) : "memory", "scc"); }
    STEP4(1); STEP4(2);
    lds_wait(lds + L_SYNC + 16 + (lane & 3) * 4, 1);
    asm volatile("s_waitcnt vmcnt(8)\n\ts_mov_b64 exec, 1\n\tglobal_store_dwordx2 %0, %1, %2 sc1\n\ts_mov_b64 exec, -1"
                 :: "v"(w < 4 ? WS_FS + (u32)((4 * qb + tq) * 16 + h) * 8 : WS_FO + (u32)(ptile * 16 + g) * 8), "v"(tag), "s"(ws) : "memory");
    if (bid == 0 && w == 0) {
#pragma unroll 1
      for (u32 spins = 0; spins < SPIN_CAP; ++spins) {
        const u32x4 fa0 = BLD(WS_FST + lane * 32, 0), fa1 = BLD(WS_FST + lane * 32, 16);
        const u32 tl = (u32)tag, th = (u32)(tag >> 32);
        if (__all(fa0[0] == tl && fa0[1] == th && fa0[2] == tl && fa0[3] == th && fa1[0] == tl && fa1[1] == th && fa1[2] == tl && fa1[3] == th)) break;
        __builtin_amdgcn_s_sleep(4);
      }
      if (lane == 0) *(GAS u64*)(ws + WS_GEN) = tag;
    }
    STEP4(3); STEP4(4);
    fav = __hip_atomic_load(fa, RLX_AGENT);
    SBAR();
    STEP4(5);
    if (__builtin_expect(!__all(fav == tag), 0)) wait_flags(ws, fao, tag);
    asm volatile("" ::: "memory");
    STEP4(6);
    lds_wait(lds + L_SYNC + 32, 8);
    STEP4(7);
    o[3] = MFMA(kv[7][3], pb, o[3]);
  }
  ST(4);
  const u32 wob = (u32)(kq * 4 * 3136 + ((lane & 15) >> 2) * 112 + ((lane >> 4) & 1) * 32 + (lane & 3) * 8);
  u32x4 wf[16];
#define RDWO(br, i) do { const LAS char* wb_ = lds + L_WO + (br) * WO_B + wob + (i) * 3136; const LAS char* wa_ = wb_ + hh * 448; \
    wf[4 * (i) + 0] = trfrag(wa_, wa_ + 896); wf[4 * (i) + 1] = trfrag(wa_ + 56, wa_ + 56 + 896); \
    wf[4 * (i) + 2] = trfrag(wa_ + 1792, wb_ + 2688); wf[4 * (i) + 3] = trfrag(wa_ + 56 + 1792, wb_ + 56 + 2688); } while (0)
  {
    const int ws1 = w & 1;
    { const u32x4 a = pk8(o[0], 0), b = pk8(o[0], 8); const u32x4 oth = ws1 ? a : b;
      ldsw128(lds + L_SCR + (u32)((w ^ 1) * 7 + 0) * 1024 + lane * 16, oth); }
#pragma unroll
    for (int i = 1; i < 4; ++i)
#pragma unroll
      for (int s2 = 0; s2 < 2; ++s2) {
        const int owner = ((i ^ (w >> 1)) << 1) | s2;
        ldsw128(lds + L_SCR + (u32)(owner * 7 + (w ^ owner) - 1) * 1024 + lane * 16, pk8(o[i], 8 * s2));
      }
    if (hh) {
#pragma unroll
      for (int i = 0; i < 4; ++i) *(LAS float*)(lds + L_LP + (u32)(((i ^ (w >> 1)) * 32 + r32) * 32 + w * 4)) = o[i][12];
    }
    f32x16 acc = o[0];
#pragma unroll
    for (int j = 0; j < 8; ++j) acc[j] = sel(ws1, o[0][8 + j], o[0][j]);
    u32x4 idf;
    { const int jj = r32 - 4 * hh;
      idf[0] = jj == 0 ? 0x3F80u : jj == 1 ? 0x3F800000u : 0u; idf[1] = jj == 2 ? 0x3F80u : jj == 3 ? 0x3F800000u : 0u;
      idf[2] = jj == 8 ? 0x3F80u : jj == 9 ? 0x3F800000u : 0u; idf[3] = jj == 10 ? 0x3F80u : jj == 11 ? 0x3F800000u : 0u; }
    PIN(acc); PIN(idf);
    asm volatile("s_waitcnt vmcnt(6)" ::: "memory");
    LBAR();
    ST(5);
    u32x4 pv[7];
#pragma unroll
    for (int k = 0; k < 7; ++k) pv[k] = ldsr128(lds + L_SCR + (u32)(w * 7 + k) * 1024 + lane * 16);
    const f32x4 la = *(const LAS f32x4*)(lds + L_LP + (u32)(((w >> 1) * 32 + r32) * 32)), lb = *(const LAS f32x4*)(lds + L_LP + (u32)(((w >> 1) * 32 + r32) * 32 + 16));
    SBAR();
#pragma unroll
    for (int k = 0; k < 7; ++k) acc = MFMA(idf, pv[k], acc);
    RDWO(0, 0);
    float l = ((la[0] + la[1]) + (la[2] + la[3])) + ((lb[0] + lb[1]) + (lb[2] + lb[3]));
    float own[8];
#pragma unroll
    for (int j = 0; j < 8; ++j) own[j] = acc[j];
    float chk = 0.f, chk2 = 0.f;
#pragma unroll
    for (int j = 0; j < 4; ++j) { chk = __builtin_fmaf(own[j], 0.f, chk); chk2 = __builtin_fmaf(own[4 + j], 0.f, chk2); }
    chk += chk2;
    const bool good = (l >= 0x1p-70f) && (l <= 0x1p126f) && (chk == 0.f);
    if (__builtin_expect(!__all(good), 0)) {
      u32x4 q0 = qf[0][0], q1 = qf[0][1];
      if (hh) q1[2] = 0;
      const u32 kb = WS_T + (u32)((16 + h) * 64 * 2) * 1024 + lane * 16;
      float m = -INFINITY;
#pragma unroll 1
      for (int t = 0; t < 64; ++t) {
        const u32x4 k0 = BLD(kb, t * 2048), k1 = BLD(kb + 1024, t * 2048);
        f32x16 s = MFMA(k0, q0, zero16); s = MFMA(k1, q1, s);
#pragma unroll
        for (int i = 0; i < 16; ++i) m = fmaxf(m, s[i]);
      }
      m = swapmax(m);
      f32x16 oo = zero16;
#pragma unroll 1
      for (int t = 0; t < 64; ++t) {
        const u32x4 k0 = BLD(kb, t * 2048), k1 = BLD(kb + 1024, t * 2048), v0 = BLD(kb + 2097152, t * 2048), v1 = BLD(kb + 2097152 + 1024, t * 2048);
        f32x16 s = MFMA(k0, q0, zero16); s = MFMA(k1, q1, s);
#pragma unroll
        for (int i = 0; i < 16; ++i) s[i] = __builtin_amdgcn_exp2f(s[i] - m);
        oo = MFMA(v0, pk8(s, 0), oo); oo = MFMA(v1, pk8(s, 8), oo);
      }
      { auto r = __builtin_amdgcn_permlane32_swap(__float_as_uint(oo[12]), __float_as_uint(oo[12]), false, false); l = __uint_as_float(r[1]); }
#pragma unroll
      for (int j = 0; j < 8; ++j) own[j] = sel(ws1, oo[8 + j], oo[j]);
    }
    const float inv = __builtin_amdgcn_rcpf(l);
    u32x4 ob = {pk(own[0] * inv, own[1] * inv), pk(own[2] * inv, own[3] * inv), pk(own[4] * inv, own[5] * inv), pk(own[6] * inv, own[7] * inv)};
    if (ws1 && hh) { ob[2] = 0; ob[3] = 0; }
#pragma unroll
    for (int j = 0; j < 4; ++j) ob[j] = min(ob[j], SENT - 1);
    BST(ob, WS_O + 2097152 + (u32)(((4 * qb + (w >> 1)) * 16 + h) * 2 + ws1) * 1024 + lane * 16);
  }
  ST(6);
  const u32 pwo = (u32)((kq * 64 + qt2 * 32 + r32) * 240 + hh * 16);
  const int trow = tid / 7, tg = tid - trow * 7;
  const u32 pro = (u32)(trow * 240 + tg * 32);
  char* op = out + (size_t)(32 * ot0 + trow) * 1792 + cg * 112 + tg * 16;
  f32x16 c0, c1;
#define MMO(F, i) do { c0 = MFMA(wf[4 * (i) + 0], F[2 * (i)], (i) == 0 ? zero16 : c0); c1 = MFMA(wf[4 * (i) + 1], F[2 * (i)], (i) == 0 ? zero16 : c1); \
    c0 = MFMA(wf[4 * (i) + 2], F[2 * (i) + 1], c0); c1 = MFMA(wf[4 * (i) + 3], F[2 * (i) + 1], c1); } while (0)
#define PUTP(base) do { _Pragma("unroll") for (int g_ = 0; g_ < 4; ++g_) if (g_ < 3 || !hh) { \
      *(LAS f32x4*)(lds + (base) + pwo + g_ * 32) = (f32x4){c0[4 * g_], c0[4 * g_ + 1], c0[4 * g_ + 2], c0[4 * g_ + 3]}; \
      *(LAS f32x4*)(lds + (base) + pwo + 112 + g_ * 32) = (f32x4){c1[4 * g_], c1[4 * g_ + 1], c1[4 * g_ + 2], c1[4 * g_ + 3]}; } } while (0)
#define SUMP(base, br) do { if (w < 7) { const LAS char* p_ = lds + (base) + pro; f32x4 r_[8]; \
      _Pragma("unroll") for (int k_ = 0; k_ < 4; ++k_) { r_[2 * k_] = *(const LAS f32x4*)(p_ + k_ * 15360); r_[2 * k_ + 1] = *(const LAS f32x4*)(p_ + k_ * 15360 + 16); } \
      const f32x4 ra_ = (r_[0] + r_[2]) + (r_[4] + r_[6]), rc_ = (r_[1] + r_[3]) + (r_[5] + r_[7]); \
      *(u32x4*)(op + (br) * 896) = (u32x4){pk(ra_[0], ra_[1]), pk(ra_[2], ra_[3]), pk(rc_[0], rc_[1]), pk(rc_[2], rc_[3])}; } } while (0)
  SBAR();
  RDWO(0, 1); SBAR(); MMO(of, 0); SBAR();
  RDWO(0, 2); SBAR(); MMO(of, 1); SBAR();
  RDWO(0, 3); SBAR(); MMO(of, 2); SBAR();
  MMO(of, 3); SBAR();
  u32x4 og[8];
#pragma unroll
  for (int i = 0; i < 8; ++i) og[i] = BLD(WS_O + 2097152 + ofo, i * 1024);
  SBAR();
  RDWO(1, 0); RDWO(1, 1); RDWO(1, 2); RDWO(1, 3);
  LBAR();
  PUTP(L_SCR);
  LBAR();
  SUMP(L_SCR, 0);
  ST(7);
#define CHECKP(i) do { _Pragma("unroll 1") for (u32 spins = 0; spins < SPIN_CAP; ++spins) { \
      const u32 mx_ = max(max(max(og[2 * (i)][0], og[2 * (i)][1]), max(og[2 * (i)][2], og[2 * (i)][3])), max(max(og[2 * (i) + 1][0], og[2 * (i) + 1][1]), max(og[2 * (i) + 1][2], og[2 * (i) + 1][3]))); \
      if (__builtin_expect(__all(mx_ != SENT), 1)) break; \
      __builtin_amdgcn_s_sleep(2); \
      og[2 * (i)] = BLD(WS_O + 2097152 + ofo, (2 * (i)) * 1024); og[2 * (i) + 1] = BLD(WS_O + 2097152 + ofo, (2 * (i) + 1) * 1024); } } while (0)
  CHECKP(0); MMO(og, 0);
  CHECKP(1); MMO(og, 1);
  CHECKP(2); MMO(og, 2);
  CHECKP(3);
  ST(8);
  MMO(og, 3);
  PUTP(0);
  LBAR();
  SUMP(0, 1);
  ST(9);
  asm volatile(".Lfa_end:");
}

extern "C" void kernel_launch(void* const* d_in, const int* in_sizes, int n_in, void* d_out, int out_size, void* d_ws, size_t ws_size,
                              hipStream_t stream) {
  fused_attn<<<256, 512, 0, stream>>>((const char*)d_in[0], (const char*)d_in[1], (const char*)d_in[2], (const char*)d_in[3],
                                      (const char*)d_in[4], (const char*)d_in[5], (char*)d_out, (char*)d_ws);
}
```

```cpp
#include <hip/hip_runtime.h>
#include <hip/hip_bf16.h>

typedef unsigned u32;
typedef unsigned long long u64;
typedef short bf16x8 __attribute__((ext_vector_type(8)));
typedef float f32x16 __attribute__((ext_vector_type(16)));
typedef float f32x4 __attribute__((ext_vector_type(4)));
typedef float f32x2 __attribute__((ext_vector_type(2)));
typedef u32 u32x4 __attribute__((ext_vector_type(4)));
typedef u32 u32x2 __attribute__((ext_vector_type(2)));
typedef __bf16 bf2_t __attribute__((ext_vector_type(2)));
#define LAS __attribute__((address_space(3)))
#define GAS __attribute__((address_space(1)))
#define DI __device__ __forceinline__
#define SBAR() __builtin_amdgcn_sched_barrier(0)
#define PIN(x) asm volatile("" : "+v"(x))
#define MFMA(a, b, c) __builtin_amdgcn_mfma_f32_32x32x16_bf16(__builtin_bit_cast(bf16x8, a), __builtin_bit_cast(bf16x8, b), (c), 0, 0, 0)
#define RLX_AGENT __ATOMIC_RELAXED, __HIP_MEMORY_SCOPE_AGENT
#define BLD(voff, soff) __builtin_amdgcn_raw_buffer_load_b128(rs, (voff), (soff), 16)
#define BST(v, voff) __builtin_amdgcn_raw_buffer_store_b128((v), rs, (voff), 0, 16)

#define POLL_SLEEP 4
#define G1PRIO 3
#define ROT 7
#define LPRIO 3
#define WOP_E 16
#define PUT_FIRST 0
#define OUT_AUXA 0
#define OUT_AUXB 0
#define XPF_STEP 1
#define PFLAG_STEP 1

constexpr u32 WS_GEN = 0;
constexpr u32 WS_FST = 2048;
constexpr u32 WS_FT = 4096;
constexpr u32 WS_FO = WS_FT + 8192;
constexpr u32 WS_FS = WS_FO + 8192;
constexpr u32 WS_FP = WS_FS + 8192;
constexpr u32 WS_T = 65536;
constexpr u32 WS_O = WS_T + 6291456;
constexpr u32 WS_P = WS_O + 2 * 2097152;
constexpr u32 WS_END = WS_P + 16 * 86016;
constexpr u32 WS_STAMP = 16u << 20;

constexpr u32 SLOT_B = 30720, SLOT_HALO = 16384, SLOT_W = 18432;
constexpr u32 L_Q = 4 * SLOT_B;
constexpr u32 L_X = L_Q + 8192;
constexpr u32 L_WO = 0, WO_B = 50176;
constexpr u32 L_SCR = 2 * WO_B;
constexpr u32 L_LP = L_SCR + 57344;
constexpr u32 L_SYNC = L_LP + 4096;
constexpr u32 LDS_BYTES = 163840;

constexpr float SCALE = 0.18898223650461362f;
constexpr float LOG2E = 1.4426950408889634f;
constexpr float QSC = SCALE * LOG2E;
constexpr float MSHIFT = 40.0f;
constexpr u32 SPIN_CAP = 1u << 17;
constexpr u32 SENT = 0xFFFFFFFFu;

DI u32 pk(float lo, float hi) { f32x2 v = {lo, hi}; return __builtin_bit_cast(u32, __builtin_convertvector(v, bf2_t)); }
DI u32x4 pk8(const f32x16& a, int b) { u32x4 r = {pk(a[b], a[b + 1]), pk(a[b + 2], a[b + 3]), pk(a[b + 4], a[b + 5]), pk(a[b + 6], a[b + 7])}; return r; }
DI float rnd16(float x) { return __uint_as_float(pk(0.f, x)); }
DI float blo(u32 x) { return __uint_as_float(x << 16); }
DI float bhi(u32 x) { return __uint_as_float(x & 0xffff0000u); }
DI float sel(bool c, float a, float b) { PIN(a); PIN(b); return c ? a : b; }
DI float swapmax(float m) { auto r = __builtin_amdgcn_permlane32_swap(__float_as_uint(m), __float_as_uint(m), false, false); return fmaxf(__uint_as_float(r[0]), __uint_as_float(r[1])); }
DI float swapadd(float m) { auto r = __builtin_amdgcn_permlane32_swap(__float_as_uint(m), __float_as_uint(m), false, false); return __uint_as_float(r[0]) + __uint_as_float(r[1]); }
#define NTS ""
DI void glds16(u32 voff, const void* sbase, u32 ldsaddr) {
  u32 keep;
  asm volatile("s_mov_b32 %0, m0\n\ts_mov_b32 m0, %3\n\ts_nop 0\n\tglobal_load_lds_dwordx4 %1, %2" NTS "\n\ts_mov_b32 m0, %0"
               : "=&s"(keep) : "v"(voff), "s"(sbase), "s"(ldsaddr) : "memory");
}
DI void glds16_sc1(u32 voff, const void* sbase, u32 ldsaddr) {
  u32 keep;
  asm volatile("s_mov_b32 %0, m0\n\ts_mov_b32 m0, %3\n\ts_nop 0\n\tglobal_load_lds_dwordx4 %1, %2 sc1\n\ts_mov_b32 m0, %0"
               : "=&s"(keep) : "v"(voff), "s"(sbase), "s"(ldsaddr) : "memory");
}
DI void touch_l2(u32 voff, const void* sbase, u32 ldsaddr) {
  u32 keep;
  asm volatile("s_mov_b32 %0, m0\n\ts_mov_b32 m0, %3\n\ts_nop 0\n\tglobal_load_lds_dword %1, %2" NTS "\n\ts_mov_b32 m0, %0"
               : "=&s"(keep) : "v"(voff), "s"(sbase), "s"(ldsaddr) : "memory");
}
DI void glds49(u32 voff, const void* sbase, u32 ldsaddr) {
  u32 keep;
  asm volatile("s_mov_b32 %0, m0\n\ts_mov_b32 m0, %3\n\ts_mov_b32 exec_hi, 0x1ffff\n\tglobal_load_lds_dwordx4 %1, %2\n\ts_mov_b32 exec_hi, -1\n\ts_mov_b32 m0, %0"
               : "=&s"(keep) : "v"(voff), "s"(sbase), "s"(ldsaddr) : "memory");
}
DI u32x4 ldsr128(const LAS char* p) { return *(const LAS u32x4*)p; }
DI void ldsw128(LAS char* p, u32x4 v) { *(LAS u32x4*)p = v; }
typedef short v4i16_t __attribute__((ext_vector_type(4)));
DI u32x2 ldstr(const LAS char* p) { return __builtin_bit_cast(u32x2, __builtin_amdgcn_ds_read_tr16_b64_v4i16((LAS v4i16_t*)p)); }
DI u32x4 trfrag(const LAS char* p0, const LAS char* p1) { u32x2 a = ldstr(p0), b = ldstr(p1); u32x4 r = {a[0], a[1], b[0], b[1]}; return r; }
#define LBAR() do { __builtin_amdgcn_s_waitcnt(0xC07F); asm volatile("s_barrier" ::: "memory"); SBAR(); } while (0)

DI void lds_set(LAS char* p) { asm volatile("" ::: "memory"); __hip_atomic_store((LAS u32*)p, 1u, __ATOMIC_RELAXED, __HIP_MEMORY_SCOPE_WORKGROUP); asm volatile("" ::: "memory"); }
DI void lds_wait(const LAS char* p, u32 want) {
#pragma unroll 1
  for (u32 spins = 0; spins < (SPIN_CAP << 4); ++spins) {
    const u32 v = __hip_atomic_load((const LAS u32*)p, __ATOMIC_RELAXED, __HIP_MEMORY_SCOPE_WORKGROUP);
    if (__all(v == want)) break;
    __builtin_amdgcn_s_sleep(1);
  }
  asm volatile("" ::: "memory");
}
DI void wait_flags(const char* ws, u32 laneoff, u64 tag) {
  const GAS u64* f = (const GAS u64*)(ws + laneoff);
#pragma unroll 1
  for (u32 spins = 0; spins < SPIN_CAP; ++spins) {
    u64 v = __hip_atomic_load(f, RLX_AGENT);
    if (__all(v == tag)) break;
    __builtin_amdgcn_s_sleep(POLL_SLEEP);
  }
  __builtin_amdgcn_fence(__ATOMIC_ACQUIRE, "wavefront");
  asm volatile("" ::: "memory");
}
DI void set_flag(char* ws, u32 off, u64 tag) { __hip_atomic_store((GAS u64*)(ws + off), tag, RLX_AGENT); }

#define ST(i) do {} while (0)

#define TP(i) do {} while (0)
#define LWAIT(N, c) asm volatile("s_waitcnt vmcnt(" #N ")\n\ts_barrier" ::: "memory")
DI void lds_add1(LAS char* p, int lane) { asm volatile("" ::: "memory"); if (lane == 0) __hip_atomic_fetch_add((LAS u32*)p, 1u, __ATOMIC_RELAXED, __HIP_MEMORY_SCOPE_WORKGROUP); asm volatile("" ::: "memory"); }
#define SLOTADDR(c) do { const u32 sb_ = (u32)((c) & 3) * SLOT_B; xa = sb_ + xa0; xh = sb_ + xh0; wp = lds + sb_ + wa0; } while (0)
#define RDX(j) x[j] = ldsr128(lds + (xa ^ ((j) << 5)))
#define RDH(j) do { if (HALO) hx[j] = ldsr128(lds + (xh ^ ((j) << 5))); } while (0)
#define RDW(t, j) f[t][j] = PACKED ? ldsr128(wp + (j) * 3072 + (t) * 1024) : trfrag(wp + (j) * 3072 + (t) * 56, wp + (j) * 3072 + 768 + (t) * 56)
#define MM(A, t, j) A = ((t) >= 3 - NV) ? MFMA(x[j], f[t][j], A) : MFMA(f[t][j], x[j], A)
#define MH(j) do { if (HALO == 1) ah = MFMA(f[1][j], hx[j], ah); if (HALO == 2) ah = MFMA(hx[j], f[2][j], ah); } while (0)
#define HALF(m, r, I0, I1, I2, I3) do { \
    MM(a0, 0, m); SBAR(); RDX(r); RDW(0, r); SBAR(); \
    MM(a1, 1, m); SBAR(); RDW(1, r); I0; SBAR(); \
    MM(a2, 2, m); SBAR(); RDW(2, r); RDH(r); I1; SBAR(); \
    MH(m); SBAR(); \
    MM(a0, 0, m + 1); SBAR(); RDX(r + 1); RDW(0, r + 1); I2; SBAR(); \
    MM(a1, 1, m + 1); SBAR(); RDW(1, r + 1); RDW(2, r + 1); RDH(r + 1); SBAR(); \
    MM(a2, 2, m + 1); SBAR(); I3; SBAR(); \
    MH(m + 1); SBAR(); } while (0)
#define RDFIRST() do { RDX(0); RDW(0, 0); RDW(1, 0); RDW(2, 0); RDH(0); RDX(1); RDW(0, 1); RDW(1, 1); RDW(2, 1); RDH(1); SBAR(); } while (0)

template <int NV>
DI void gemmB(LAS char* lds, u32 xa0, u32 wa0, f32x16& a0, f32x16& a1, f32x16& a2, const char* ws, int fstep, u32 flagoff, u64 tag, int lane) {
  constexpr bool PACKED = false; constexpr int HALO = 0;
  u32x4 x[4], f[3][4], hx[4]; f32x16 ah;
  u32 xa, xh; const u32 xh0 = 0; const LAS char* wp;
  LBAR();
  SLOTADDR(0);
  RDFIRST();
#pragma unroll 1
  for (int c = 0; c < 7; ++c) {
    HALF(0, 2, , , , );
    asm volatile("s_cmp_eq_u32 %0, %1\n\ts_cbranch_scc0 1f\n\ts_waitcnt vmcnt(0)\n\ts_mov_b64 exec, 3\n\tglobal_store_dwordx2 %2, %3, %4 sc1\n\ts_mov_b64 exec, -1\n1:"
                 :: "s"(c), "s"(fstep), "v"(flagoff), "v"(tag), "s"(ws) : "memory", "scc");
    __builtin_amdgcn_s_waitcnt(0xC07F);
    asm volatile("s_cmp_eq_u32 %0, 6\n\ts_cbranch_scc1 1f\n\ts_barrier\n1:" :: "s"(c) : "memory", "scc");
    SBAR();
    SLOTADDR(c + 1);
    HALF(2, 0, , , , );
  }
}

template <int HALO>
DI void gemmA(LAS char* lds, u32 xa0, u32 xh0, u32 wa0, f32x16& a0, f32x16& a1, f32x16& a2, f32x16& ah) {
  constexpr bool PACKED = true; constexpr int NV = 1;
  u32x4 x[4], f[3][4], hx[4];
  u32 xa, xh; const LAS char* wp;
  SLOTADDR(7);
  RDFIRST();
#pragma unroll 1
  for (int c = 7; c < 14; ++c) {
    HALF(0, 2, , , , );
    __builtin_amdgcn_s_waitcnt(0xC07F);
    asm volatile("s_waitcnt vmcnt(7)\n\ts_cmp_eq_u32 %0, 7\n\ts_cbranch_scc1 2f\n\ts_waitcnt vmcnt(0)\n\ts_cmp_eq_u32 %0, 13\n\ts_cbranch_scc1 1f\n2:\n\ts_barrier\n1:" :: "s"(c) : "memory", "scc");
    SBAR();
    SLOTADDR(c + 1);
    HALF(2, 0, , , , );
  }
}

__global__ __launch_bounds__(512, 2) void fused_attn(const char* __restrict__ Ax, const char* __restrict__ Aw, const char* __restrict__ Ao,
                                                     const char* __restrict__ Bx, const char* __restrict__ Bw, const char* __restrict__ Bo,
                                                     char* __restrict__ out, char* ws) {
  __shared__ __attribute__((aligned(16))) char lds_raw[LDS_BYTES];
  LAS char* lds = (LAS char*)lds_raw;
  const u32 lds0 = (u32)(size_t)lds;
  const int tid = threadIdx.x, lane = tid & 63, r32 = lane & 31, hh = lane >> 5;
  const int w = __builtin_amdgcn_readfirstlane(tid >> 6);
  const int bid = blockIdx.x, xcd = bid & 7, slot = bid >> 3;
  const int rb = 4 * (xcd & 3) + (slot & 3), g = 8 * (xcd >> 2) + (slot >> 2);
  const int h = 2 * xcd + (slot >> 4), qb = slot & 15;
  const int cg = slot & 7, ot0 = 8 * xcd + 2 * (slot >> 3);
  const int tq = w & 3;
  asm volatile("" :: "s"(Ax), "s"(Aw), "s"(Ao), "s"(Bx), "s"(Bw), "s"(Bo), "s"(out), "s"(ws));
  ST(0);
  u64 stv = 0;
  const bool packer = w < 3 && rb < 14;
  if (packer) {
    const u32 vo = (u32)(32 * rb + (lane >> 2)) * 2688 + tq * 896 + 56 * g - 8 * (g & 1) + (lane & 3) * 16;
    glds16(vo, Aw, lds0 + L_X + tq * 2560); glds16(vo, Aw + 16 * 2688, lds0 + L_X + tq * 2560 + 1024);
  }
  int kc = (g & 7) + 2 * (rb & 3); kc = kc >= 7 ? kc - 7 : kc;
  const int p0 = (tq & 1) + 8 * (tq >> 1);
  const u32 voffX = (u32)(128 * rb + 8 * p0 + (lane >> 3)) * 896u + (u32)(((lane & 7) ^ ((4 * (tq & 1) + (lane >> 4)) & 7)) << 4);
  { const int j0 = (w >> 2) * 2;
    glds16(voffX, Bx + kc * 128 + j0 * 14336, lds0 + (p0 + 2 * j0) * 1024);
    glds16(voffX, Bx + kc * 128 + (j0 + 1) * 14336, lds0 + (p0 + 2 * j0 + 2) * 1024); }
  u32 voffW[3];
#pragma unroll
  for (int j = 0; j < 3; ++j) { const int i = 64 * j + lane, kr = (i * 171) >> 11, rem = i - kr * 12; voffW[j] = (16 * tq + kr) * 2688 + 168 * g - 8 * (g & 1) + min(rem, 10) * 16; }
  if (w >= 4) { glds16(voffW[0], Bw + kc * (64 * 2688), lds0 + SLOT_W + (3 * tq) * 1024); glds16(voffW[1], Bw + kc * (64 * 2688), lds0 + SLOT_W + (3 * tq + 1) * 1024); }
  else glds16(voffW[2], Bw + kc * (64 * 2688), lds0 + SLOT_W + (3 * tq + 2) * 1024);
  kc = kc == 6 ? 0 : kc + 1;

  if (w == 3) {
    u64 pc; u32 len;
    asm volatile("s_getpc_b64 %0\n.Lfa_pc:\n\ts_mov_b32 %1, .Lfa_end-.Lfa_pc" : "=s"(pc), "=s"(len));
    touch_l2(min((u32)(slot + 32 * (lane & 15)) * 128u, len), (const void*)pc, lds0 + L_SYNC + 1024);
  }
  const __amdgpu_buffer_rsrc_t rs = __builtin_amdgcn_make_buffer_rsrc(ws, 0, WS_END, 0x00020000);
  const f32x16 zero16 = {};
  u64 tag;
  u32x4 qf[4][2];
  u32x4 kv[8][4];
  const u32 kvb = WS_T + (u32)(((16 + h) * 64 + 8 * w) * 2) * 1024 + lane * 16;
  const int trot = qb >> 1;
  const int ptile = 4 * rb + tq;
#define LOADF(t, f) kv[t][f] = BLD(kvb + ((f) & 1) * 1024, (((t) + trot) & 7) * 2048 + ((f) >> 1) * 2097152)
#define LOADT(t) do { LOADF(t, 0); LOADF(t, 1); LOADF(t, 2); LOADF(t, 3); } while (0)
#define READQ() do { _Pragma("unroll") for (int i_ = 0; i_ < 4; ++i_) { const u32 a_ = L_Q + (u32)((i_ ^ (w >> 1)) * 2048) + lane * 16; \
      qf[i_][0] = ldsr128(lds + a_); qf[i_][1] = ldsr128(lds + a_ + 1024); } } while (0)
#define GETTAG() do { const u64 gen_ = *(const __attribute__((address_space(4))) u64*)(ws + WS_GEN); \
      tag = gen_ * 6364136223846793005ull + 1442695040888963407ull; asm volatile("" : "+s"(tag)); } while (0)
  u32 voffH;
  { const int hr = 8 * (tq & 1) + (lane >> 3); int tok = 128 * rb - 16 + hr; tok = tok < 0 ? 0 : tok;
    voffH = (u32)tok * 896u + (u32)(((lane & 7) ^ ((hr >> 1) & 7)) << 4); }
  auto issueA = [&](int cc) {
    const u32 sb = lds0 + (u32)(cc & 3) * SLOT_B;
    const char* xs = Ax + kc * 128; const char* wq = ws + WS_P + g * 86016 + kc * 12288 + tq * 3072;
    kc = kc == 6 ? 0 : kc + 1;
#pragma unroll
    for (int j = 0; j < 4; ++j) glds16(voffX, xs + j * 14336, sb + (p0 + 2 * j) * 1024);
    if (tq < 2) glds16(voffH, xs, sb + SLOT_HALO + tq * 1024);
#pragma unroll
    for (int j = 0; j < 3; ++j) glds16_sc1((u32)lane * 16, wq + j * 1024, sb + SLOT_W + (3 * tq + j) * 1024);
  };
  const int xrow = 32 * tq + r32;
  const u32 xa0 = xrow * 128 + ((hh ^ ((xrow >> 1) & 7)) << 4);
  const u32 xh0 = SLOT_HALO + (r32 & 15) * 128 + ((hh ^ (((r32 & 15) >> 1) & 7)) << 4);
  f32x16 aq = zero16, ak = zero16, av = zero16, ah = zero16;

  if (w >= 4) {
    auto issueB = [&](int cc) {
      const u32 sb = lds0 + (u32)(cc & 3) * SLOT_B;
      const char* xs = Bx + kc * 128; const char* wq = Bw + kc * (64 * 2688);
      kc = kc == 6 ? 0 : kc + 1;
#pragma unroll
      for (int j = 0; j < 4; ++j) {
        glds16(voffX, xs + j * 14336, sb + (p0 + 2 * j) * 1024);
        if (j < 3) glds16(voffW[j], wq, sb + SLOT_W + (3 * tq + j) * 1024);
      }
    };
#define XTOUCH() do { int row0 = 128 * rb - 16 + 18 * (g & 7); row0 = row0 < 0 ? 0 : row0; \
      touch_l2((u32)row0 * 896u + (u32)min(tq * 32 + r32, 125) * 128u, Ax, lds0 + L_SYNC + 1024); } while (0)
    issueB(1);
    if (XPF_STEP < 0) XTOUCH();
    GETTAG();
    const u32 pflag = WS_FP + (u32)(g * 64 + (lane < 42 ? lane : 0)) * 8;
    u64 pfv = 0;
#pragma unroll 1
    for (int c = 0; c < 5; ++c) {
      { u32 t_; asm volatile("s_sub_u32 %0, %1, %2\n\ts_cmp_lt_u32 %0, 2\n\ts_cbranch_scc1 1f\n\ts_waitcnt vmcnt(7)\n1:" : "=&s"(t_) : "s"(c), "n"(XPF_STEP + 1) : "memory", "scc"); }
      LWAIT(8, c);
      asm volatile("s_cmp_eq_u32 %3, %4\n\ts_cbranch_scc0 1f\n\tglobal_load_dwordx2 %0, %1, %2 sc1\n1:" : "+v"(pfv) : "v"(pflag), "s"(ws), "s"(c), "s"(w) : "memory", "scc");
      issueB(c + 2);
      if (c == XPF_STEP) XTOUCH();
    }
    asm volatile("s_waitcnt vmcnt(7)" : "+v"(pfv) :: "memory");
    if (w == 4 && !__all(pfv == tag)) wait_flags(ws, pflag, tag);
    LWAIT(7, 5);
    issueA(7);
    LWAIT(7, 6);
    issueA(8); issueA(9);
    ST(1);
    asm volatile("s_waitcnt vmcnt(14)\n\ts_barrier" ::: "memory");
    { const u32 wp0 = SLOT_W + lane * 16;
      if (tq == 2) gemmA<1>(lds, xa0, xh0, wp0, aq, ak, av, ah);
      else if (tq == 3) gemmA<2>(lds, xa0, xh0, wp0, aq, ak, av, ah);
      else gemmA<0>(lds, xa0, xh0, wp0, aq, ak, av, ah); }
    ST(2);
    TP(60);
    __builtin_amdgcn_s_setprio(LPRIO);
    LAS char* xb = lds + L_X;
    u32x4 aq0 = pk8(aq, 0), aq1 = pk8(aq, 8), ak0 = pk8(ak, 0), ak1 = pk8(ak, 8), cv0 = pk8(av, 0), cv1 = pk8(av, 8);
    if (hh) { aq1[2] = 0; aq1[3] = 0; ak1[2] = 0; ak1[3] = 0; }
    if (r32 >= 28) { cv0 = (u32x4){0, 0, 0, 0}; cv1 = cv0; }
    if (tq < 3) { LAS char* p = xb + (tq + 1) * 3072 + lane * 16; ldsw128(p, ak0); ldsw128(p + 1024, ak1); ldsw128(p + 2048, cv1); }
    if (tq == 2) {
      u32x4 h0 = pk8(ah, 0), h1 = pk8(ah, 8);
      if (hh) { h1[2] = 0; h1[3] = 0; }
      ldsw128(xb + lane * 16, h0); ldsw128(xb + 1024 + lane * 16, h1);
    } else if (tq == 3) {
      u32x4 h1 = pk8(ah, 8);
      if (r32 >= 28) h1 = (u32x4){0, 0, 0, 0};
      ldsw128(xb + 2048 + lane * 16, h1);
    }
    lds_set(lds + L_SYNC + 16 + tq * 4);
    f32x16 sc = MFMA(ak0, aq0, zero16); sc = MFMA(ak1, aq1, sc);
    lds_wait(lds + L_SYNC + 16 + (lane & 3) * 4, 1);
    u32x4 pk0 = ldsr128(xb + tq * 3072 + lane * 16), pk1 = ldsr128(xb + tq * 3072 + 1024 + lane * 16), pv1 = ldsr128(xb + tq * 3072 + 2048 + lane * 16);
    f32x16 sp = MFMA(pk0, aq0, zero16); sp = MFMA(pk1, aq1, sp);
    const bool odd = r32 & 1;
    float t[4]; int kk[4];
#pragma unroll
    for (int j = 0; j < 4; ++j) {
      kk[j] = ((r32 & ~1) + 16 - (2 * (j & 1) + 8 * (j >> 1) + 4 * hh)) >> 4;
      const float s0 = sel(odd, sp[8 + 2 * j + 1], sp[8 + 2 * j]), s1 = sel(odd, sc[2 * j + 1], sc[2 * j]), s2 = sel(odd, sc[8 + 2 * j + 1], sc[8 + 2 * j]);
      const float sv = kk[j] == 0 ? s0 : kk[j] == 1 ? s1 : s2;
      const float lg = rnd16(rnd16(sv) * SCALE);
      t[j] = (kk[j] == 0 && ptile == 0) ? -INFINITY : lg;
    }
    const float mx = swapmax(fmaxf(fmaxf(t[0], t[1]), fmaxf(t[2], t[3])));
#pragma unroll
    for (int j = 0; j < 4; ++j) t[j] = __builtin_amdgcn_exp2f((t[j] - mx) * LOG2E);
    const float inv = __builtin_amdgcn_rcpf(swapadd((t[0] + t[1]) + (t[2] + t[3])));
    u32x4 pp, p0, p1;
#pragma unroll
    for (int j = 0; j < 4; ++j) {
      const u32 b = pk(t[j] * inv, 0.f) << (odd ? 16 : 0);
      pp[j] = kk[j] == 0 ? b : 0u; p0[j] = kk[j] == 1 ? b : 0u; p1[j] = kk[j] == 2 ? b : 0u;
    }
    f32x16 oa = MFMA(cv0, p0, zero16); oa = MFMA(cv1, p1, oa); oa = MFMA(pv1, pp, oa);
    lds_wait(lds + L_SYNC + 44, 1);
    LOADT(0); LOADT(1);
    SBAR();
    BST(pk8(oa, 0), WS_O + (u32)(ptile * 16 + g) * 2048 + lane * 16);
    BST(pk8(oa, 8), WS_O + (u32)(ptile * 16 + g) * 2048 + 1024 + lane * 16);
    lds_wait(lds + L_SYNC + (lane & 3) * 4, 1);
    READQ();
  } else {
    if (tid < 16) *(LAS u32*)(lds + L_SYNC + tid * 4) = 0;
    GETTAG();
    asm volatile("s_waitcnt vmcnt(3)" ::: "memory");
    if (packer) {
      const LAS char* pp = lds + L_X + tq * 2560 + (8 * hh + ((lane & 15) >> 2)) * 64 + 8 * (g & 1) + ((lane >> 4) & 1) * 32 + (lane & 3) * 8;
      const u32x4 f0 = trfrag(pp, pp + 256), f1 = trfrag(pp + 1024, pp + 1280);
      const u32 po = WS_P + (u32)(g * 84 + 6 * rb + tq) * 1024 + lane * 16;
      BST(f0, po); BST(f1, po + 3072);
      asm volatile("s_waitcnt vmcnt(2)" ::: "memory");
    } else asm volatile("s_waitcnt vmcnt(0)" ::: "memory");
    { const u32 wa0 = SLOT_W + (8 * hh + ((lane & 15) >> 2)) * 192 + 8 * (g & 1) + ((lane >> 4) & 1) * 32 + (lane & 3) * 8;
      const int fs = (packer || w == 0) ? PFLAG_STEP : 99;
      const u32 fo = (packer && !(w == 0 && lane == 1)) ? WS_FP + (u32)(g * 64 + 3 * rb + tq) * 8 : WS_FST + (u32)bid * 8;
      if (g < 10) gemmB<0>(lds, xa0, wa0, aq, ak, av, ws, fs, fo, tag, lane);
      else if (g == 10) gemmB<1>(lds, xa0, wa0, aq, ak, av, ws, fs, fo, tag, lane);
      else gemmB<3>(lds, xa0, wa0, aq, ak, av, ws, fs, fo, tag, lane); }
    ST(1);
    __builtin_amdgcn_s_setprio(3);
    const u32 to = WS_T + (u32)((3 * g * 64 + ptile) * 2) * 1024 + lane * 16;
    auto put = [&](f32x16& a, int j) {
      const int sl = 3 * g + j;
      if (sl < 16) a *= QSC;
      u32x4 t0 = pk8(a, 0), t1 = pk8(a, 8);
      if (sl < 32) { if (hh) { t1[2] = sl < 16 ? 0u : 0x00003F80u; t1[3] = 0; } }
      else if (r32 >= 28) { const u32 f = r32 == 28 ? 0x3F803F80u : 0u; t0 = (u32x4){f, f, f, f}; t1 = t0; }
      BST(t0, to + j * 131072); BST(t1, to + j * 131072 + 1024);
    };
    kc = kc >= 5 ? kc - 5 : kc + 2;
    __builtin_amdgcn_s_waitcnt(0xC07F);
    LWAIT(63, 7);
    put(aq, 0); put(ak, 1); put(av, 2);
    LWAIT(63, 8);
    LWAIT(63, 9);
    issueA(10);
    asm volatile("s_waitcnt vmcnt(7)" ::: "memory");
    u32 prevs;
    { u32 prev = 0;
      if (lane == 0) prev = __hip_atomic_fetch_add((LAS u32*)(lds + L_SYNC + 40), 1u, __ATOMIC_RELAXED, __HIP_MEMORY_SCOPE_WORKGROUP);
      prevs = __builtin_amdgcn_readfirstlane(prev);
      if (prevs == 3 && lane < 3) set_flag(ws, WS_FT + (u32)((3 * g + lane) * 16 + rb) * 8, tag); }
    issueA(11);
    const u32 tflag = WS_FT + (u32)((lane & 32) ? h * 16 + qb : (16 + (lane & 16) + h) * 16 + (lane & 15)) * 8;
    u64 tfv;
    asm volatile("s_cmp_eq_u32 %0, 3\n\ts_cbranch_scc1 1f\n\ts_waitcnt vmcnt(7)\n1:" :: "s"(prevs) : "memory", "scc");
    LWAIT(8, 10); issueA(12);
    LWAIT(7, 11); issueA(13);
    LWAIT(7, 12);
    asm volatile("global_load_dwordx2 %0, %1, %2 sc1" : "=v"(tfv) : "v"(tflag), "s"(ws) : "memory");
    LWAIT(1, 13);
    TP(28);
    asm volatile("s_waitcnt vmcnt(0)" : "+v"(tfv) :: "memory");
    if (!__all(tfv == tag)) {
      if (w == 0) wait_flags(ws, tflag, tag); else lds_wait(lds + L_SYNC + 44, 1);
    }
    if (w == 0) lds_set(lds + L_SYNC + 44);
    TP(29);
    u32x4 q0 = BLD(WS_T + (u32)((h * 64 + 4 * qb + tq) * 2) * 1024 + lane * 16, 0), q1 = BLD(WS_T + (u32)((h * 64 + 4 * qb + tq) * 2) * 1024 + lane * 16, 1024);
    LOADT(0); LOADT(1);
    SBAR();
    f32x16 s = MFMA(kv[0][0], q0, zero16); s = MFMA(kv[0][1], q1, s);
    ldsw128(lds + L_Q + tq * 2048 + lane * 16, q0);
    float m = s[0];
#pragma unroll
    for (int i = 1; i < 16; ++i) m = fmaxf(m, s[i]);
    m = swapmax(m);
    if (hh) q1[2] = pk(-(m + MSHIFT), 0.f);
    ldsw128(lds + L_Q + tq * 2048 + 1024 + lane * 16, q1);
    lds_set(lds + L_SYNC + tq * 4);
    ST(2);
    TP(30);
    lds_wait(lds + L_SYNC + (lane & 3) * 4, 1);
    READQ();
    TP(31);
  }
  __builtin_amdgcn_s_waitcnt(0xC07F);
  if (lane == 0) __hip_atomic_fetch_add((LAS u32*)(lds + L_SYNC + 32), 1u, __ATOMIC_RELAXED, __HIP_MEMORY_SCOPE_WORKGROUP);
  ST(3);
  const int qt2 = w & 1, kq = w >> 1, otile = ot0 + qt2;
  const u32 ofo = (u32)(otile * 16 + 4 * kq) * 2048 + lane * 16;
  u32x4 of[8];
  f32x16 o[4];
  {
    const u32 voffO = (u32)(min(lane / 7, 6) * 3584 + (lane % 7) * 16 + cg * 112);
    const char* wosrc = (w < 4 ? Ao : Bo) + tq * (112 * 896);
    const u32 wodst = lds0 + L_WO + w * 12544;
#define WOP(j) do { const int q_ = (((j) >> 2) + (slot >> 3)) & 3; glds49(voffO, wosrc + (((j) & 3) * 28 + q_) * 896, wodst + (q_ * 4 + ((j) & 3)) * 784); } while (0)
    const u32 fao = WS_FO + (u32)(lane & 4) * 2048 + (u32)(otile * 16 + 4 * kq + (lane & 3)) * 8;
    u64 fav;
    f32x16 s[2]; u32x4 pa, pb;
    __builtin_amdgcn_s_setprio(2);
    SBAR();
    s[0] = MFMA(kv[0][0], qf[0][0], zero16); s[0] = MFMA(kv[0][1], qf[0][1], s[0]);
    SBAR();
#define EXP(x) x = __builtin_amdgcn_exp2f(x)
#define STEP(u) do { \
      constexpr int kt = (u) >> 2, qt = (u) & 3, un = (u) + 1, ktn = (un >> 2) & 7, qtn = un & 3, up = (u) > 0 ? (u) - 1 : 0, kl = (kt + 3) & 7; \
      f32x16& sc = s[(u) & 1]; f32x16& sn = s[un & 1]; \
      if (un < 32) { sn = MFMA(kv[ktn][0], qf[qtn][0], zero16); PIN(sn); } SBAR(); \
      if ((u) < 8) { LOADF(2 + 2 * kt, qt); LOADF(3 + 2 * kt, qt); } else if ((u) < 16) LOADF((kt + 4) & 7, qt); \
      if ((u) >= 24) of[(u) & 7] = BLD(WS_O + ofo, ((u) & 7) * 1024); \
      EXP(sc[0]); EXP(sc[1]); EXP(sc[2]); EXP(sc[3]); PIN(sc); SBAR(); \
      if ((u) > 0) { o[up & 3] = MFMA(kv[up >> 2][3], pb, o[up & 3]); PIN(o[up & 3]); } SBAR(); \
      EXP(sc[4]); EXP(sc[5]); PIN(sc); SBAR(); pa[0] = pk(sc[0], sc[1]); pa[1] = pk(sc[2], sc[3]); PIN(pa); SBAR(); EXP(sc[6]); EXP(sc[7]); PIN(sc); SBAR(); \
      if (un < 32) { sn = MFMA(kv[ktn][1], qf[qtn][1], sn); PIN(sn); } SBAR(); \
      if ((u) >= 4 && (u) < WOP_E) WOP((u) - 4); if ((u) >= 20 && (u) < 40 - WOP_E) WOP((u) - 24 + WOP_E); \
      EXP(sc[8]); PIN(sc); SBAR(); pa[2] = pk(sc[4], sc[5]); pa[3] = pk(sc[6], sc[7]); PIN(pa); SBAR(); EXP(sc[9]); EXP(sc[10]); EXP(sc[11]); PIN(sc); SBAR(); \
      o[qt] = MFMA(kv[kt][2], pa, (u) < 4 ? zero16 : o[qt]); PIN(o[qt]); SBAR(); \
      EXP(sc[12]); EXP(sc[13]); EXP(sc[14]); EXP(sc[15]); PIN(sc); SBAR(); \
      pb[0] = pk(sc[8], sc[9]); pb[1] = pk(sc[10], sc[11]); pb[2] = pk(sc[12], sc[13]); pb[3] = pk(sc[14], sc[15]); PIN(pb); SBAR(); \
      if ((u) == 15) __builtin_amdgcn_s_setprio(1); \
      if ((u) == 26) __builtin_amdgcn_s_setprio(0); \
    } while (0)
#define STEP4(t) TP(32 + (w >> 2) * 12 + (t)); STEP(4 * (t)); STEP(4 * (t) + 1); STEP(4 * (t) + 2); STEP(4 * (t) + 3)
    STEP4(0);
    const u32 myflag = w < 4 ? WS_FS + (u32)((4 * qb + tq) * 16 + h) * 8 : WS_FO + (u32)(ptile * 16 + g) * 8;
    {
      const u32x4 sv = {SENT, SENT, SENT, SENT};
      asm volatile("s_cmp_lt_u32 %0, 4\n\ts_cbranch_scc0 1f\n\tbuffer_store_dwordx4 %1, %2, %3, 0 offen sc1\n\tbuffer_store_dwordx4 %1, %2, %3, 0 offen offset:1024 sc1\n\ts_nop 1\n\ts_branch 2f\n"
                   "1:\n\ts_waitcnt vmcnt(8)\n\ts_mov_b64 exec, 1\n\tglobal_store_dwordx2 %4, %5, %6 sc1\n\ts_mov_b64 exec, -1\n2:"
                   :: "s"(w), "v"(sv), "v"(WS_O + 2097152 + (u32)((4 * qb + tq) * 16 + h) * 2048 + lane * 16), "s"(rs), "v"(myflag), "v"(tag), "s"(ws) : "memory", "scc"); }
    lds_wait(lds + L_SYNC + 16 + (lane & 3) * 4, 1);
    STEP4(1); STEP4(2);
    asm volatile("s_cmp_lt_u32 %0, 4\n\ts_cbranch_scc0 1f\n\ts_waitcnt vmcnt(20)\n\ts_mov_b64 exec, 1\n\tglobal_store_dwordx2 %1, %2, %3 sc1\n\ts_mov_b64 exec, -1\n1:"
                 :: "s"(w), "v"(myflag), "v"(tag), "s"(ws) : "memory", "scc");
    if (bid == 0 && w == 0) {
#pragma unroll 1
      for (u32 spins = 0; spins < SPIN_CAP; ++spins) {
        const u32x4 fa0 = BLD(WS_FST + lane * 32, 0), fa1 = BLD(WS_FST + lane * 32, 16);
        const u32 tl = (u32)tag, th = (u32)(tag >> 32);
        if (__all(fa0[0] == tl && fa0[1] == th && fa0[2] == tl && fa0[3] == th && fa1[0] == tl && fa1[1] == th && fa1[2] == tl && fa1[3] == th)) break;
        __builtin_amdgcn_s_sleep(4);
      }
      if (lane == 0) *(GAS u64*)(ws + WS_GEN) = tag;
    }
    STEP4(3); STEP4(4);
    __builtin_amdgcn_s_waitcnt(0x0F70);
    asm volatile("global_load_dwordx2 %0, %1, %2 sc1" : "=v"(fav) : "v"(fao), "s"(ws) : "memory");
    SBAR();
    STEP4(5);
    asm volatile("s_waitcnt vmcnt(4)" : "+v"(fav) :: "memory");
    if (__builtin_expect(!__all(fav == tag), 0)) wait_flags(ws, fao, tag);
    asm volatile("" ::: "memory");
    STEP4(6);
    lds_wait(lds + L_SYNC + 32, 8);
    STEP4(7);
    o[3] = MFMA(kv[7][3], pb, o[3]);
  }
  ST(4);
  TP(32 + (w >> 2) * 12 + 8);
  const u32 wob = (u32)(kq * 12544 + ((lane & 15) >> 2) * 3136 + ((lane >> 4) & 1) * 32 + (lane & 3) * 8);
  u32x4 wf[16];
#define RDWO(br, i) do { const LAS char* wb_ = lds + L_WO + (br) * WO_B + wob + (i) * 784; const LAS char* wa_ = wb_ + hh * 112; \
    wf[4 * (i) + 0] = trfrag(wa_, wa_ + 224); wf[4 * (i) + 1] = trfrag(wa_ + 56, wa_ + 56 + 224); \
    wf[4 * (i) + 2] = trfrag(wa_ + 448, wb_ + 672); wf[4 * (i) + 3] = trfrag(wa_ + 56 + 448, wb_ + 56 + 672); } while (0)
  {
    const int ws1 = w & 1;
    { const u32x4 a = pk8(o[0], 0), b = pk8(o[0], 8); const u32x4 oth = ws1 ? a : b;
      ldsw128(lds + L_SCR + (u32)((w ^ 1) * 7 + 0) * 1024 + lane * 16, oth); }
#pragma unroll
    for (int i = 1; i < 4; ++i)
#pragma unroll
      for (int s2 = 0; s2 < 2; ++s2) {
        const int owner = ((i ^ (w >> 1)) << 1) | s2;
        ldsw128(lds + L_SCR + (u32)(owner * 7 + (w ^ owner) - 1) * 1024 + lane * 16, pk8(o[i], 8 * s2));
      }
    if (hh) {
#pragma unroll
      for (int i = 0; i < 4; ++i) *(LAS float*)(lds + L_LP + (u32)(((i ^ (w >> 1)) * 32 + r32) * 32 + w * 4)) = o[i][12];
    }
    f32x16 acc = o[0];
#pragma unroll
    for (int j = 0; j < 8; ++j) acc[j] = sel(ws1, o[0][8 + j], o[0][j]);
    u32x4 idf;
    { const int jj = r32 - 4 * hh;
      idf[0] = jj == 0 ? 0x3F80u : jj == 1 ? 0x3F800000u : 0u; idf[1] = jj == 2 ? 0x3F80u : jj == 3 ? 0x3F800000u : 0u;
      idf[2] = jj == 8 ? 0x3F80u : jj == 9 ? 0x3F800000u : 0u; idf[3] = jj == 10 ? 0x3F80u : jj == 11 ? 0x3F800000u : 0u; }
    PIN(acc); PIN(idf);
    asm volatile("s_waitcnt vmcnt(%0)" :: "n"(WOP_E - 8) : "memory");
    LBAR();
    ST(5);
    u32x4 pv[7];
#pragma unroll
    for (int k = 0; k < 7; ++k) pv[k] = ldsr128(lds + L_SCR + (u32)(w * 7 + k) * 1024 + lane * 16);
    const f32x4 la = *(const LAS f32x4*)(lds + L_LP + (u32)(((w >> 1) * 32 + r32) * 32)), lb = *(const LAS f32x4*)(lds + L_LP + (u32)(((w >> 1) * 32 + r32) * 32 + 16));
    SBAR();
#pragma unroll
    for (int k = 0; k < 7; ++k) acc = MFMA(idf, pv[k], acc);
    RDWO(0, 0);
    float l = ((la[0] + la[1]) + (la[2] + la[3])) + ((lb[0] + lb[1]) + (lb[2] + lb[3]));
    float own[8];
#pragma unroll
    for (int j = 0; j < 8; ++j) own[j] = acc[j];
    float chk = 0.f, chk2 = 0.f;
#pragma unroll
    for (int j = 0; j < 4; ++j) { chk = __builtin_fmaf(own[j], 0.f, chk); chk2 = __builtin_fmaf(own[4 + j], 0.f, chk2); }
    chk += chk2;
    const bool good = (l >= 0x1p-70f) && (l <= 0x1p126f) && (chk == 0.f);
    if (__builtin_expect(!__all(good), 0)) {
      u32x4 q0 = qf[0][0], q1 = qf[0][1];
      if (hh) q1[2] = 0;
      const u32 kb = WS_T + (u32)((16 + h) * 64 * 2) * 1024 + lane * 16;
      float m = -INFINITY;
#pragma unroll 1
      for (int t = 0; t < 64; ++t) {
        const u32x4 k0 = BLD(kb, t * 2048), k1 = BLD(kb + 1024, t * 2048);
        f32x16 s = MFMA(k0, q0, zero16); s = MFMA(k1, q1, s);
#pragma unroll
        for (int i = 0; i < 16; ++i) m = fmaxf(m, s[i]);
      }
      m = swapmax(m);
      f32x16 oo = zero16;
#pragma unroll 1
      for (int t = 0; t < 64; ++t) {
        const u32x4 k0 = BLD(kb, t * 2048), k1 = BLD(kb + 1024, t * 2048), v0 = BLD(kb + 2097152, t * 2048), v1 = BLD(kb + 2097152 + 1024, t * 2048);
        f32x16 s = MFMA(k0, q0, zero16); s = MFMA(k1, q1, s);
#pragma unroll
        for (int i = 0; i < 16; ++i) s[i] = __builtin_amdgcn_exp2f(s[i] - m);
        oo = MFMA(v0, pk8(s, 0), oo); oo = MFMA(v1, pk8(s, 8), oo);
      }
      { auto r = __builtin_amdgcn_permlane32_swap(__float_as_uint(oo[12]), __float_as_uint(oo[12]), false, false); l = __uint_as_float(r[1]); }
#pragma unroll
      for (int j = 0; j < 8; ++j) own[j] = sel(ws1, oo[8 + j], oo[j]);
    }
    const float inv = __builtin_amdgcn_rcpf(l);
    u32x4 ob = {pk(own[0] * inv, own[1] * inv), pk(own[2] * inv, own[3] * inv), pk(own[4] * inv, own[5] * inv), pk(own[6] * inv, own[7] * inv)};
    if (ws1 && hh) { ob[2] = 0; ob[3] = 0; }
#pragma unroll
    for (int j = 0; j < 4; ++j) ob[j] = min(ob[j], SENT - 1);
    BST(ob, WS_O + 2097152 + (u32)(((4 * qb + (w >> 1)) * 16 + h) * 2 + ws1) * 1024 + lane * 16);
  }
  ST(6);
  const u32 pwo = (u32)((kq * 64 + qt2 * 32 + r32) * 240 + hh * 16);
  const int trow = tid / 7, tg = tid - trow * 7;
  const u32 pro = (u32)(trow * 240 + tg * 32);
  const __amdgpu_buffer_rsrc_t rso = __builtin_amdgcn_make_buffer_rsrc(out, 0, 2048 * 1792, 0x00020000);
  const u32 opo = (u32)((32 * ot0 + trow) * 1792 + cg * 112 + tg * 16);
  f32x16 c0, c1;
#define MMO(F, i) do { c0 = MFMA(wf[4 * (i) + 0], F[2 * (i)], (i) == 0 ? zero16 : c0); c1 = MFMA(wf[4 * (i) + 1], F[2 * (i)], (i) == 0 ? zero16 : c1); \
    c0 = MFMA(wf[4 * (i) + 2], F[2 * (i) + 1], c0); c1 = MFMA(wf[4 * (i) + 3], F[2 * (i) + 1], c1); } while (0)
#define PUTP(base) do { _Pragma("unroll") for (int g_ = 0; g_ < 4; ++g_) if (g_ < 3 || !hh) { \
      *(LAS f32x4*)(lds + (base) + pwo + g_ * 32) = (f32x4){c0[4 * g_], c0[4 * g_ + 1], c0[4 * g_ + 2], c0[4 * g_ + 3]}; \
      *(LAS f32x4*)(lds + (base) + pwo + 112 + g_ * 32) = (f32x4){c1[4 * g_], c1[4 * g_ + 1], c1[4 * g_ + 2], c1[4 * g_ + 3]}; } } while (0)
#define SUMP(base, br) do { if (w < 7) { const LAS char* p_ = lds + (base) + pro; f32x4 r_[8]; \
      _Pragma("unroll") for (int k_ = 0; k_ < 4; ++k_) { r_[2 * k_] = *(const LAS f32x4*)(p_ + k_ * 15360); r_[2 * k_ + 1] = *(const LAS f32x4*)(p_ + k_ * 15360 + 16); } \
      const f32x4 ra_ = (r_[0] + r_[2]) + (r_[4] + r_[6]), rc_ = (r_[1] + r_[3]) + (r_[5] + r_[7]); \
      __builtin_amdgcn_raw_buffer_store_b128((u32x4){pk(ra_[0], ra_[1]), pk(ra_[2], ra_[3]), pk(rc_[0], rc_[1]), pk(rc_[2], rc_[3])}, rso, opo + (br) * 896, 0, (br) ? OUT_AUXB : OUT_AUXA); } } while (0)
  SBAR();
  RDWO(0, 1); SBAR(); MMO(of, 0); SBAR();
  RDWO(0, 2); SBAR(); MMO(of, 1); SBAR();
  RDWO(0, 3); SBAR(); MMO(of, 2); SBAR();
  MMO(of, 3); SBAR();
  u32x4 og[8];
#pragma unroll
  for (int i = 0; i < 8; ++i) og[i] = BLD(WS_O + 2097152 + ofo, i * 1024);
  SBAR();
  RDWO(1, 0); RDWO(1, 1); RDWO(1, 2); RDWO(1, 3);
  LBAR();
  PUTP(L_SCR);
  LBAR();
  SUMP(L_SCR, 0);
  ST(7);
#define CHECKP(i) do { _Pragma("unroll 1") for (u32 spins = 0; spins < SPIN_CAP; ++spins) { \
      const u32 mx_ = max(max(max(og[2 * (i)][0], og[2 * (i)][1]), max(og[2 * (i)][2], og[2 * (i)][3])), max(max(og[2 * (i) + 1][0], og[2 * (i) + 1][1]), max(og[2 * (i) + 1][2], og[2 * (i) + 1][3]))); \
      if (__builtin_expect(__all(mx_ != SENT), 1)) break; \
      __builtin_amdgcn_s_sleep(2); \
      og[2 * (i)] = BLD(WS_O + 2097152 + ofo, (2 * (i)) * 1024); og[2 * (i) + 1] = BLD(WS_O + 2097152 + ofo, (2 * (i) + 1) * 1024); } } while (0)
  CHECKP(0); MMO(og, 0);
  CHECKP(1); MMO(og, 1);
  CHECKP(2); MMO(og, 2);
  CHECKP(3);
  ST(8);
  MMO(og, 3);
  PUTP(0);
  LBAR();
  SUMP(0, 1);
  ST(9);
  asm volatile(".Lfa_end:");
}

extern "C" void kernel_launch(void* const* d_in, const int* in_sizes, int n_in, void* d_out, int out_size, void* d_ws, size_t ws_size,
                              hipStream_t stream) {
  fused_attn<<<256, 512, 0, stream>>>((const char*)d_in[0], (const char*)d_in[1], (const char*)d_in[2], (const char*)d_in[3],
                                      (const char*)d_in[4], (const char*)d_in[5], (char*)d_out, (char*)d_ws);
}
```
